```python
import torch

def _softmax(x, axis=-1):
    return torch.softmax(x, dim=axis)

def _gelu(x):
    return torch.nn.functional.gelu(x, approximate='tanh')

def _silu(x):
    return torch.nn.functional.silu(x)

def _sigmoid(x):
    return torch.sigmoid(x)

def _layernorm(x, axis=-1, eps=1e-05):
    m = x.mean(dim=axis, keepdim=True)
    v = x.var(dim=axis, keepdim=True, unbiased=False)
    return (x - m) / torch.sqrt(v + eps)

def _scatter_add(tgt, idx, src, axis=0):
    return tgt.clone().index_add_(axis, idx, src)

def _segment_sum(src, seg, n):
    out = torch.zeros((n,) + src.shape[1:], dtype=src.dtype)
    return out.index_add_(0, seg, src)

def _pad(x, pw):
    flat = tuple((v for pair in reversed(pw) for v in pair))
    return torch.nn.functional.pad(x, flat)
kernel_name = 'adaLN_cond__mha_block_cached__glu_gated_swiglu__decode'

def setup_inputs():

    _w = lambda a: (lambda t: t.to(torch.bfloat16) if t.is_floating_point() else t)(torch.as_tensor(a))

    def _A_setup():
        B, Sq, D = (2, 1, 512)
        H, Sk, Dh = (32, 8192, 128)
        return {'x': _w((lambda *s: torch.randn(*s, dtype=torch.bfloat16) * 0.1)(B, Sq, D)), 'k_cache': _w((lambda *s: torch.randn(*s, dtype=torch.bfloat16) * 0.8)(B, H, Sk, Dh)), 'v_cache': _w((lambda *s: torch.randn(*s, dtype=torch.bfloat16) * 3.2)(B, H, Sk, Dh)), 'wq': _w((lambda *s: torch.randn(*s, dtype=torch.bfloat16) * 0.8)(D, H * Dh) * D ** (-0.5)), 'wo': _w((lambda *s: torch.randn(*s, dtype=torch.bfloat16) * 3.2)(H * Dh, D) * (H * Dh) ** (-0.5))}
    _A_in = _A_setup()

    def _B_setup():
        B, S, D, F = (2, 1, 512, 1376)
        return {'x': _w((lambda *s: torch.randn(*s, dtype=torch.bfloat16) * 0.1)(B, S, D)), 'w1': _w((lambda *s: torch.randn(*s, dtype=torch.bfloat16) * 3.2)(D, F) * (2.0 / D) ** 0.5), 'w3': _w((lambda *s: torch.randn(*s, dtype=torch.bfloat16) * 3.2)(D, F) * (2.0 / D) ** 0.5), 'w2': _w((lambda *s: torch.randn(*s, dtype=torch.bfloat16) * 3.2)(F, D) * (2.0 / F) ** 0.5)}
    _B_in = _B_setup()
    return {'A_x': _A_in['x'], 'A_k_cache': _A_in['k_cache'], 'A_v_cache': _A_in['v_cache'], 'A_wq': _A_in['wq'], 'A_wo': _A_in['wo'], 'B_x': _B_in['x'], 'B_w1': _B_in['w1'], 'B_w3': _B_in['w3'], 'B_w2': _B_in['w2'], 'c': _w((lambda *s: torch.randn(*s, dtype=torch.bfloat16) * 0.2)(2, 512) * 8.0), 'w_mod': _w((lambda *s: torch.randn(*s, dtype=torch.bfloat16) * 0.2)(512, 6 * 512) * 512 ** (-0.5) * 8.0)}

def reference(A_x, A_k_cache, A_v_cache, A_wq, A_wo, B_x, B_w1, B_w3, B_w2, c, w_mod):

    def _A_ref(x, k_cache, v_cache, wq, wo):
        B, Sq, D = x.shape
        H, Dh = (32, 128)
        q = (lambda x, *d: x.permute(*d))((x @ wq).reshape(B, Sq, H, Dh), 0, 2, 1, 3)
        scores = torch.einsum('bhqd,bhkd->bhqk', q, k_cache) * Dh ** (-0.5)
        p = _softmax(scores, axis=-1)
        o = torch.einsum('bhqk,bhkd->bhqd', p, v_cache)
        return (lambda x, *d: x.permute(*d))(o, 0, 2, 1, 3).reshape(B, Sq, H * Dh) @ wo

    def _B_ref(x, w1, w3, w2):
        gate = torch.einsum('bsd,df->bsf', x, w1)
        up = torch.einsum('bsd,df->bsf', x, w3)
        h = _silu(gate) * up
        out = torch.einsum('bsf,fd->bsd', h, w2)
        return out
    x = A_x
    mod = (_silu(c) @ w_mod).reshape(c.shape[0], 6, 1, -1)
    sa, ca, ga, sb, cb, gb = (mod[:, 0].clone(), mod[:, 1].clone(), mod[:, 2].clone(), mod[:, 3].clone(), mod[:, 4].clone(), mod[:, 5].clone())
    h = _layernorm(x, axis=-1) * (1 + ca) + sa
    x = x + ga * _A_ref(h, A_k_cache, A_v_cache, A_wq, A_wo)
    h = _layernorm(x, axis=-1) * (1 + cb) + sb
    x = x + gb * _B_ref(h, B_w1, B_w3, B_w2)
    return x
```

```cpp
#include <hip/hip_runtime.h>
#include <hip/hip_bf16.h>
#include <stdint.h>

typedef unsigned short u16;
typedef unsigned v4u __attribute__((ext_vector_type(4)));
using bf16x2 = __attribute__((ext_vector_type(2))) __bf16;

constexpr int B = 2, D = 512, H = 32, SK = 8192, DH = 128, F = 1376;
constexpr int NSPLIT = 8;

__device__ __forceinline__ float bf2f(u16 b) { return __uint_as_float((unsigned)b << 16); }
__device__ __forceinline__ float rbf(float f) {
  return __bfloat162float(__float2bfloat16(f));
}
__device__ __forceinline__ float dot2(unsigned k, unsigned q, float acc) {
  return __builtin_amdgcn_fdot2_f32_bf16(__builtin_bit_cast(bf16x2, k), __builtin_bit_cast(bf16x2, q), acc, false);
}
__device__ __forceinline__ float block_sum(float v, float* red) {
  for (int o = 32; o > 0; o >>= 1) v += __shfl_xor(v, o);
  __syncthreads();
  if ((threadIdx.x & 63) == 0) red[threadIdx.x >> 6] = v;
  __syncthreads();
  float s = 0.f;
  for (int i = 0; i < (int)(blockDim.x >> 6); ++i) s += red[i];
  return s;
}

__global__ void k_mod(const u16* c, const u16* wmod, float* mod) {
  __shared__ float sc[2][512];
  __shared__ float red[4][2][64];
  for (int i = threadIdx.x; i < 1024; i += 256) { float v = bf2f(c[i]); sc[i >> 9][i & 511] = rbf(v / (1.f + __expf(-v))); }
  __syncthreads();
  int jj = threadIdx.x & 63, kq = threadIdx.x >> 6, j = blockIdx.x * 64 + jj;
  float a0 = 0, a1 = 0;
  for (int i = kq * 128; i < kq * 128 + 128; ++i) { float w = bf2f(wmod[i * 3072 + j]); a0 += sc[0][i] * w; a1 += sc[1][i] * w; }
  red[kq][0][jj] = a0; red[kq][1][jj] = a1;
  __syncthreads();
  if (threadIdx.x < 128) { int b = threadIdx.x >> 6; float s = red[0][b][jj] + red[1][b][jj] + red[2][b][jj] + red[3][b][jj]; mod[b * 3072 + j] = rbf(s); }
}

__device__ __forceinline__ float ln_mod(float xv, float shift, float scale, float* red) {
  float m = rbf(block_sum(xv, red) * (1.f / D));
  float mean_exact = block_sum(xv, red) * (1.f / D);
  float dv = xv - mean_exact;
  float v = rbf(block_sum(dv * dv, red) * (1.f / D));
  float n = rbf(rbf(xv - m) / rbf(sqrtf(rbf(v + 1e-5f))));
  return rbf(rbf(n * rbf(1.f + scale)) + shift);
}

__global__ void k_q(const u16* x, const float* mod, const u16* wq, u16* q) {
  __shared__ float sh[2][512];
  __shared__ float red[8];
  __shared__ float r2[8][2][64];
  int t = threadIdx.x;
  for (int b = 0; b < 2; ++b) sh[b][t] = ln_mod(bf2f(x[b * 512 + t]), mod[b * 3072 + t], mod[b * 3072 + 512 + t], red);
  __syncthreads();
  int jj = t & 63, kq = t >> 6, j = blockIdx.x * 64 + jj;
  float a0 = 0, a1 = 0;
  for (int i = kq * 64; i < kq * 64 + 64; ++i) { float w = bf2f(wq[i * 4096 + j]); a0 += sh[0][i] * w; a1 += sh[1][i] * w; }
  r2[kq][0][jj] = a0; r2[kq][1][jj] = a1;
  __syncthreads();
  if (t < 128) { int b = t >> 6; float s = 0; for (int k = 0; k < 8; ++k) s += r2[k][b][jj];
    q[b * 4096 + j] = __builtin_bit_cast(u16, __float2bfloat16(s)); }
}

__global__ void __launch_bounds__(256) k_attn(const u16* q, const u16* K, const u16* V, float* pm, float* pl, float* po) {
  __shared__ float sm[4], sl[4], so[4][128];
  int s = blockIdx.x, bh = blockIdx.y, t = threadIdx.x, lane = t & 63, w = t >> 6, r = lane >> 4, c = lane & 15;
  constexpr int KPS = SK / NSPLIT, KPW = KPS / 4;
  v4u qv = *(const v4u*)(q + bh * 128 + 8 * c);
  const v4u* kp = (const v4u*)(K + ((size_t)bh * SK + s * KPS + w * KPW) * DH) + lane;
  const v4u* vp = (const v4u*)(V + ((size_t)bh * SK + s * KPS + w * KPW) * DH) + lane;
  float m = -INFINITY, l = 0.f, acc[8] = {0, 0, 0, 0, 0, 0, 0, 0};
  for (int it = 0; it < KPW / 16; ++it) {
    v4u kk[4], vv[4];
#pragma unroll
    for (int i = 0; i < 4; ++i) { kk[i] = __builtin_nontemporal_load(kp + (it * 4 + i) * 64); vv[i] = __builtin_nontemporal_load(vp + (it * 4 + i) * 64); }
    float sc[4];
#pragma unroll
    for (int i = 0; i < 4; ++i) { float d = 0; for (int j = 0; j < 4; ++j) d = dot2(kk[i][j], qv[j], d);
      for (int o = 8; o > 0; o >>= 1) d += __shfl_xor(d, o);
      sc[i] = rbf(rbf(d) * 0.08838834764831845f); }
    float mx = fmaxf(fmaxf(sc[0], sc[1]), fmaxf(sc[2], sc[3]));
    float mn = fmaxf(m, mx), al = __expf(m - mn);
    l *= al; for (int j = 0; j < 8; ++j) acc[j] *= al;
#pragma unroll
    for (int i = 0; i < 4; ++i) { float p = __expf(sc[i] - mn); l += p;
      for (int j = 0; j < 4; ++j) { acc[2 * j] += p * __uint_as_float(vv[i][j] << 16); acc[2 * j + 1] += p * __uint_as_float(vv[i][j] & 0xffff0000u); } }
    m = mn;
  }
  float M = fmaxf(m, __shfl_xor(m, 16)); M = fmaxf(M, __shfl_xor(M, 32));
  float al = __expf(m - M); l *= al; l += __shfl_xor(l, 16); l += __shfl_xor(l, 32);
  for (int j = 0; j < 8; ++j) { acc[j] *= al; acc[j] += __shfl_xor(acc[j], 16); acc[j] += __shfl_xor(acc[j], 32); }
  if (lane == 0) { sm[w] = M; sl[w] = l; }
  if (r == 0) for (int j = 0; j < 8; ++j) so[w][8 * c + j] = acc[j];
  __syncthreads();
  if (t < 128) { float MM = fmaxf(fmaxf(sm[0], sm[1]), fmaxf(sm[2], sm[3])); float L = 0, o = 0;
    for (int i = 0; i < 4; ++i) { float e = __expf(sm[i] - MM); L += e * sl[i]; o += e * so[i][t]; }
    po[(bh * NSPLIT + s) * 128 + t] = o; if (t == 0) { pm[bh * NSPLIT + s] = MM; pl[bh * NSPLIT + s] = L; } }
}

__global__ void k_wo(const float* pm, const float* pl, const float* po, const u16* wo, float* y) {
  __shared__ float so[2][128];
  int h = blockIdx.x, t = threadIdx.x;
  if (t < 256) { int b = t >> 7, d = t & 127, bh = b * 32 + h; float M = -INFINITY;
    for (int s = 0; s < NSPLIT; ++s) M = fmaxf(M, pm[bh * NSPLIT + s]);
    float L = 0, o = 0; for (int s = 0; s < NSPLIT; ++s) { float e = __expf(pm[bh * NSPLIT + s] - M); L += e * pl[bh * NSPLIT + s]; o += e * po[(bh * NSPLIT + s) * 128 + d]; }
    so[b][d] = rbf(o / L); }
  __syncthreads();
  float a0 = 0, a1 = 0;
  for (int d = 0; d < 128; ++d) { float w = bf2f(wo[(h * 128 + d) * 512 + t]); a0 += so[0][d] * w; a1 += so[1][d] * w; }
  y[(h * 2 + 0) * 512 + t] = a0; y[(h * 2 + 1) * 512 + t] = a1;
}

__global__ void k_ffn1(const u16* x, const float* mod, const float* y, const u16* w1, const u16* w3, const u16* w2, float* x1o, float* outp) {
  __shared__ float sh[2][512];
  __shared__ float red[8];
  __shared__ float r2[16][4][32];
  __shared__ float hh[2][32];
  int t = threadIdx.x;
  for (int b = 0; b < 2; ++b) { float a = 0; for (int h = 0; h < 32; ++h) a += y[(h * 2 + b) * 512 + t];
    float x1 = rbf(bf2f(x[b * 512 + t]) + rbf(mod[b * 3072 + 1024 + t] * rbf(a)));
    if (blockIdx.x == 0) x1o[b * 512 + t] = x1;
    sh[b][t] = ln_mod(x1, mod[b * 3072 + 1536 + t], mod[b * 3072 + 2048 + t], red); }
  __syncthreads();
  int jj = t & 31, kq = t >> 5, j = blockIdx.x * 32 + jj;
  float g0 = 0, g1 = 0, u0 = 0, u1 = 0;
  for (int i = kq * 32; i < kq * 32 + 32; ++i) { float a = bf2f(w1[i * F + j]), b = bf2f(w3[i * F + j]); g0 += sh[0][i] * a; g1 += sh[1][i] * a; u0 += sh[0][i] * b; u1 += sh[1][i] * b; }
  r2[kq][0][jj] = g0; r2[kq][1][jj] = g1; r2[kq][2][jj] = u0; r2[kq][3][jj] = u1;
  __syncthreads();
  if (t < 64) { int b = t >> 5; float g = 0, u = 0; for (int k = 0; k < 16; ++k) { g += r2[k][b][jj]; u += r2[k][2 + b][jj]; }
    g = rbf(g); u = rbf(u); hh[b][jj] = rbf(rbf(g / (1.f + __expf(-g))) * u); }
  __syncthreads();
  float a0 = 0, a1 = 0;
  for (int f = 0; f < 32; ++f) { float w = bf2f(w2[(blockIdx.x * 32 + f) * 512 + t]); a0 += hh[0][f] * w; a1 += hh[1][f] * w; }
  outp[(blockIdx.x * 2 + 0) * 512 + t] = a0; outp[(blockIdx.x * 2 + 1) * 512 + t] = a1;
}

__global__ void k_fin(const float* mod, const float* x1, const float* outp, u16* out) {
  int t = blockIdx.x * 64 + threadIdx.x, b = t >> 9, n = t & 511;
  float a = 0; for (int g = 0; g < 43; ++g) a += outp[(g * 2 + b) * 512 + n];
  float r = x1[t] + rbf(mod[b * 3072 + 2560 + n] * rbf(a));
  out[t] = __builtin_bit_cast(u16, __float2bfloat16(r));
}

extern "C" void kernel_launch(void* const* d_in, const int* in_sizes, int n_in, void* d_out, int out_size, void* d_ws, size_t ws_size, hipStream_t stream) {
  const u16* x = (const u16*)d_in[0]; const u16* K = (const u16*)d_in[1]; const u16* V = (const u16*)d_in[2];
  const u16* wq = (const u16*)d_in[3]; const u16* wo = (const u16*)d_in[4];
  const u16* w1 = (const u16*)d_in[6]; const u16* w3 = (const u16*)d_in[7]; const u16* w2 = (const u16*)d_in[8];
  const u16* c = (const u16*)d_in[9]; const u16* wmod = (const u16*)d_in[10];
  float* ws = (float*)d_ws;
  float* mod = ws;
  u16* q = (u16*)(ws + 8192);
  float* pm = ws + 16384; float* pl = pm + 1024; float* po = ws + 32768;
  float* y = ws + 131072;
  float* x1 = ws + 200000; float* outp = ws + 210000;
  k_mod<<<48, 256, 0, stream>>>(c, wmod, mod);
  k_q<<<64, 512, 0, stream>>>(x, mod, wq, q);
  k_attn<<<dim3(NSPLIT, 64), 256, 0, stream>>>(q, K, V, pm, pl, po);
  k_wo<<<32, 512, 0, stream>>>(pm, pl, po, wo, y);
  k_ffn1<<<43, 512, 0, stream>>>(x, mod, y, w1, w3, w2, x1, outp);
  k_fin<<<16, 64, 0, stream>>>(mod, x1, outp, (u16*)d_out);
}
```

```cpp
#include <hip/hip_runtime.h>
#include <stdint.h>
#include <type_traits>

typedef unsigned short u16;
typedef unsigned int u32;
typedef unsigned long long u64;
typedef u32 v4u __attribute__((ext_vector_type(4)));
typedef u32 v2u __attribute__((ext_vector_type(2)));
typedef float v4f __attribute__((ext_vector_type(4)));
typedef float v2f __attribute__((ext_vector_type(2)));
typedef __bf16 bf16x2 __attribute__((ext_vector_type(2)));
typedef __bf16 bf16x8 __attribute__((ext_vector_type(8)));
typedef short v4s __attribute__((ext_vector_type(4)));
typedef __amdgpu_buffer_rsrc_t rsrc_t;
#define GAS __attribute__((address_space(1)))
#define LAS __attribute__((address_space(3)))

constexpr int KSTART = 80;
constexpr int PACE = 945;
constexpr int DEPTH = 8, DEPTH_G = 10;
constexpr int LAG = 7;
constexpr int PAUSE_G = 12, PAUSE_F = 320, PAUSE_R = 150, FAST = 600;
constexpr int MOD_T = 320, STRIP_T = 810, WO_T = 1900, WO_DT = 70, MOD_K = 1, ML_K = 6;
constexpr int D_Q = 30, D_Y = 12, D_X = 20, D_XR = 17, D_O = 18;
constexpr u32 SPIN_TICKS = 400000;
constexpr int CODE_KB = 29;
constexpr int due_of(int g) { return KSTART + ((g * PACE) >> 4); }
constexpr int slot_of(int g) { return g < 20 ? g - 8 : (g - 20) & 7; }
constexpr int used_after(int g) { return (g <= 21 || g - LAG < 11) ? 11 : g - LAG; }

constexpr int NFFN = 86, REP_X1 = 4;
constexpr u32 WS_EPOCH = 0;
constexpr u32 WS_MP = 256;
constexpr u32 WS_MOD = WS_MP + 16 * 8 * 1024;
constexpr u32 WS_ML = WS_MOD + 2 * 8192;
constexpr u32 WS_T = WS_ML + 4096;
constexpr u32 WS_X1 = WS_T + 128;
constexpr u32 WS_QP = WS_X1 + REP_X1 * 4096;
constexpr u32 WS_Y = WS_QP + 256 * 1024;
constexpr u32 WS_OP = WS_Y + 32 * 256 * 64;
constexpr u32 WS_END = WS_OP + 32 * 96 * 128;
constexpr u32 OOB = 0x7ffffff0u;
constexpr int AUX_SC1 = 16, AUX_NT = 2;

struct Params {
  const u16 *x, *K, *V, *wq, *wo, *w1, *w3, *w2, *c, *wmod;
  u16* out;
  unsigned char* ws;
};

enum { C_MP, C_Q, C_MAX, C_SUM, C_MOD, C_QP, C_QPUB, C_SHIFT };
struct Lds {
  u32 cnt[16];
  v2f red1[4][64];
  v4f red1b[4][16];
  u32 qd[64];
  float mw[4], lw[4];
  float op[4][128];
  float red2[64][4];
  u32 h2[2][256];
  float ry[4][16], ro[4][32];
  v4u stage[4][64];
  v4u kst[4][32][64];
};

__device__ __forceinline__ float bflo(u32 v) { return __uint_as_float(v << 16); }
__device__ __forceinline__ float bfhi(u32 v) { return __uint_as_float(v & 0xffff0000u); }
__device__ __forceinline__ v2f unpack2(u32 v) { return v2f{bflo(v), bfhi(v)}; }
__device__ __forceinline__ u32 pack2(float a, float b) { bf16x2 r = {(__bf16)a, (__bf16)b}; return __builtin_bit_cast(u32, r); }
__device__ __forceinline__ float rbf(float a) { return bflo(pack2(a, a)); }
__device__ __forceinline__ v2f rbf2(v2f v) { u32 r = pack2(v.x, v.y); asm("" : "+v"(r)); return unpack2(r); }
__device__ __forceinline__ float dot2(u32 a, u32 b, float acc) {
  return __builtin_amdgcn_fdot2_f32_bf16(__builtin_bit_cast(bf16x2, a), __builtin_bit_cast(bf16x2, b), acc, false);
}
__device__ __forceinline__ float silu(float v) { return v * __builtin_amdgcn_rcpf(1.f + __expf(-v)); }
__device__ __forceinline__ u32 silu2(u32 v) { return pack2(silu(bflo(v)), silu(bfhi(v))); }
template <int CTRL> __device__ __forceinline__ u32 dppu(u32 x) { return (u32)__builtin_amdgcn_mov_dpp((int)x, CTRL, 0xf, 0xf, true); }
template <int CTRL> __device__ __forceinline__ float dppf(float x) { return __uint_as_float(dppu<CTRL>(__float_as_uint(x))); }
constexpr int XOR1 = 0xB1, XOR2 = 0x4E, HMIR = 0x141, ROR4 = 0x124, ROR8 = 0x128, BCAST = 0x150;
__device__ __forceinline__ float sum32(float a, float b) {
  auto s = __builtin_amdgcn_permlane32_swap(__float_as_uint(a), __float_as_uint(b), false, false);
  return __uint_as_float(s[0]) + __uint_as_float(s[1]);
}
__device__ __forceinline__ float sum16(float a, float b) {
  auto s = __builtin_amdgcn_permlane16_swap(__float_as_uint(a), __float_as_uint(b), false, false);
  return __uint_as_float(s[0]) + __uint_as_float(s[1]);
}
template <int CTRL> __device__ __forceinline__ float trstep(float lo, float hi, bool bit) {
  const float a = lo + dppf<CTRL>(lo), b = hi + dppf<CTRL>(hi);
  return bit ? b : a;
}
__device__ __forceinline__ float wave_sum(float x) {
  x = sum32(x, x); x = sum16(x, x);
  x += dppf<ROR8>(x); x += dppf<HMIR>(x); x += dppf<XOR2>(x); x += dppf<XOR1>(x);
  return x;
}
__device__ __forceinline__ float wave_max(float x) {
  x = fmaxf(x, dppf<XOR1>(x)); x = fmaxf(x, dppf<XOR2>(x)); x = fmaxf(x, dppf<HMIR>(x)); x = fmaxf(x, dppf<ROR8>(x));
  auto s = __builtin_amdgcn_permlane16_swap(__float_as_uint(x), __float_as_uint(x), false, false);
  x = fmaxf(__uint_as_float(s[0]), __uint_as_float(s[1]));
  auto t = __builtin_amdgcn_permlane32_swap(__float_as_uint(x), __float_as_uint(x), false, false);
  return fmaxf(__uint_as_float(t[0]), __uint_as_float(t[1]));
}
__device__ __forceinline__ rsrc_t mkrs(const void* p, u32 bytes) { return __builtin_amdgcn_make_buffer_rsrc((void*)p, 0, bytes, 0x00020000); }
__device__ __forceinline__ v4u ldw(rsrc_t rs, u32 voff, u32 soff) { return __builtin_amdgcn_raw_buffer_load_b128(rs, voff, soff, AUX_NT); }
__device__ __forceinline__ u32 now() { return (u32)__builtin_amdgcn_s_memrealtime(); }
template <class T> __device__ __forceinline__ void pin(T& v) { asm volatile("" : "+v"(v)); }
template <int I, int E> struct Unroll {
  template <class F> static __device__ __forceinline__ void run(F&& f) {
    if constexpr (I < E) { f(std::integral_constant<int, I>{}); Unroll<I + 1, E>::run(f); }
  }
};

__device__ __forceinline__ void lds_arrive(LAS u32* c) {
  asm volatile("s_mov_b64 exec, 1\n\tds_add_u32 %0, %1\n\ts_mov_b64 exec, -1" :: "v"((u32)(uintptr_t)c), "v"(1u) : "memory");
}
__device__ __forceinline__ void lds_wait(LAS u32* c, u32 n) {
  while ((u32)__builtin_amdgcn_readfirstlane(__hip_atomic_load(c, __ATOMIC_RELAXED, __HIP_MEMORY_SCOPE_WORKGROUP)) < n) __builtin_amdgcn_s_sleep(1);
  asm volatile("" ::: "memory");
}
__device__ __forceinline__ bool last_of(LAS u32* c, u32 n, int lane) {
  asm volatile("" ::: "memory");
  u32 old = 0;
  if (lane == 0) old = __hip_atomic_fetch_add(c, 1u, __ATOMIC_RELAXED, __HIP_MEMORY_SCOPE_WORKGROUP);
  const bool last = (u32)__builtin_amdgcn_readfirstlane(old) == n - 1u;
  asm volatile("" ::: "memory");
  return last;
}
__device__ __forceinline__ void wg_barrier() { asm volatile("s_waitcnt lgkmcnt(0)\n\ts_barrier" ::: "memory"); }

__device__ __forceinline__ void publish(rsrc_t rs, u32 off, u32 a, u32 b, u32 tag) {
  __builtin_amdgcn_raw_buffer_store_b128(v4u{a, tag, b, tag}, rs, off, 0, AUX_SC1);
}
__device__ __forceinline__ void publish(rsrc_t rs, u32 off, v2f v, u32 tag) { publish(rs, off, __float_as_uint(v.x), __float_as_uint(v.y), tag); }
template <int N, int SLEEP, int NFULL = N> __device__ __forceinline__ void gather(rsrc_t rs, const u32 (&off)[N], v4u (&val)[N], u32 tag, u32 t0) {
  for (;;) {
#pragma unroll
    for (int k = 0; k < N; ++k) val[k] = __builtin_amdgcn_raw_buffer_load_b128(rs, off[k], 0, AUX_SC1);
    u32 bad = 0u;
#pragma unroll
    for (int k = 0; k < N; ++k) {
      const u32 m = (val[k].y ^ tag) | (val[k].w ^ tag);
      bad |= (k >= NFULL && off[k] == OOB) ? 0u : m;
    }
    asm("" : "+v"(bad));
    if (__builtin_expect(!__any(bad != 0u), 1) || now() - t0 > SPIN_TICKS) break;
    if (SLEEP) __builtin_amdgcn_s_sleep(SLEEP);
  }
}
__device__ __forceinline__ v2f payload(const v4u& u) { return v2f{__uint_as_float(u.x), __uint_as_float(u.z)}; }

__device__ __forceinline__ void interleave(v4u& a, v4u& b) {
  const v4u lo = {__builtin_amdgcn_perm(b.x, a.x, 0x05040100u), __builtin_amdgcn_perm(b.x, a.x, 0x07060302u),
                  __builtin_amdgcn_perm(b.y, a.y, 0x05040100u), __builtin_amdgcn_perm(b.y, a.y, 0x07060302u)};
  const v4u hi = {__builtin_amdgcn_perm(b.z, a.z, 0x05040100u), __builtin_amdgcn_perm(b.z, a.z, 0x07060302u),
                  __builtin_amdgcn_perm(b.w, a.w, 0x05040100u), __builtin_amdgcn_perm(b.w, a.w, 0x07060302u)};
  a = lo; b = hi;
}
__device__ __forceinline__ void rows2(v4u a, v4u b, u32 s0, u32 s1, float (&v)[16]) {
  interleave(a, b);
#pragma unroll
  for (int j = 0; j < 4; ++j) {
    v[j] = dot2(a[j], s0, v[j]); v[4 + j] = dot2(b[j], s0, v[4 + j]);
    v[8 + j] = dot2(a[j], s1, v[8 + j]); v[12 + j] = dot2(b[j], s1, v[12 + j]);
  }
}
template <int LANE> __device__ __forceinline__ void dot8(const v4u& wa, const v4u& wb, u32 vp, float* a) {
#define DOT(i, j) "v_dot2c_f32_bf16_dpp %" #i ", %8, %" #j " row_newbcast:%c17 row_mask:0xf bank_mask:0xf\n\t"
  asm(DOT(0, 9) DOT(1, 10) DOT(2, 11) DOT(3, 12) DOT(4, 13) DOT(5, 14) DOT(6, 15) DOT(7, 16)
      : "+v"(a[0]), "+v"(a[1]), "+v"(a[2]), "+v"(a[3]), "+v"(a[4]), "+v"(a[5]), "+v"(a[6]), "+v"(a[7])
      : "v"(vp), "v"(wa.x), "v"(wa.y), "v"(wa.z), "v"(wa.w), "v"(wb.x), "v"(wb.y), "v"(wb.z), "v"(wb.w), "i"(LANE));
#undef DOT
}
__device__ __forceinline__ void dpp_ready(u32& vp) { asm("s_nop 1" : "+v"(vp)); }
__device__ __forceinline__ void dots_done(float* a) {
  asm("s_nop 2" : "+v"(a[0]), "+v"(a[1]), "+v"(a[2]), "+v"(a[3]), "+v"(a[4]), "+v"(a[5]), "+v"(a[6]), "+v"(a[7]));
}
template <int NK> __device__ __forceinline__ void gemv(const v4u (&W)[32], u32 vp, float (&u)[4]) {
  float a[8] = {0.f, 0.f, 0.f, 0.f, 0.f, 0.f, 0.f, 0.f};
  dpp_ready(vp);
  Unroll<0, NK>::run([&](auto K) { dot8<K>(W[2 * K], W[2 * K + 1], vp, a); });
  dots_done(a);
#pragma unroll
  for (int j = 0; j < 4; ++j) u[j] = sum32(a[j], a[j + 4]);
}

__device__ __forceinline__ void ln_stats(const v2f (&e)[8], int lane, v2f& mean, v2f& rstd) {
  const v2f piv = {__uint_as_float(__builtin_amdgcn_readlane(__float_as_uint(e[0].x), 0)), __uint_as_float(__builtin_amdgcn_readlane(__float_as_uint(e[0].y), 0))};
  v2f d[8];
#pragma unroll
  for (int j = 0; j < 8; ++j) d[j] = e[j] - piv;
  const v2f s = ((d[0] + d[1]) + (d[2] + d[3])) + ((d[4] + d[5]) + (d[6] + d[7]));
  v2f qa = d[0] * d[0], qb = d[1] * d[1];
#pragma unroll
  for (int j = 2; j < 8; j += 2) { qa += d[j] * d[j]; qb += d[j + 1] * d[j + 1]; }
  qa += qb;
  float a = sum16(sum32(s.x, s.y), sum32(qa.x, qa.y));
  a += dppf<ROR8>(a); a += dppf<HMIR>(a); a += dppf<XOR2>(a); a += dppf<XOR1>(a);
  const auto t = __builtin_amdgcn_permlane16_swap(__float_as_uint(a), __float_as_uint(a), false, false);
  const float md = __uint_as_float(t[0]) * (1.f / 512.f), var = fmaxf(fmaf(-md, md, __uint_as_float(t[1]) * (1.f / 512.f)), 0.f);
  const v2f mv = rbf2(v2f{md + (lane < 32 ? piv.x : piv.y), var});
  const float rs = __builtin_amdgcn_rcpf(rbf(__builtin_amdgcn_sqrtf(rbf(mv.y + 1e-5f))));
  const auto m2 = __builtin_amdgcn_permlane32_swap(__float_as_uint(mv.x), __float_as_uint(mv.x), false, false);
  const auto r2 = __builtin_amdgcn_permlane32_swap(__float_as_uint(rs), __float_as_uint(rs), false, false);
  mean = v2f{__uint_as_float(m2[0]), __uint_as_float(m2[1])};
  rstd = v2f{__uint_as_float(r2[0]), __uint_as_float(r2[1])};
}
__device__ __forceinline__ v2f modulate(v2f x, v2f mean, v2f rstd, v2f scale1, v2f shift) { return rbf2(rbf2(rbf2(x - mean) * rstd) * scale1) + shift; }

__device__ __forceinline__ float qk(const v4u k, const v4u q) { return dot2(k.w, q.w, dot2(k.z, q.z, dot2(k.y, q.y, dot2(k.x, q.x, 0.f)))); }
__device__ __forceinline__ float tile_scores(float (&x)[16], int lane) {
#pragma unroll
  for (int j = 0; j < 8; ++j) x[j] = trstep<ROR8>(x[j], x[j + 8], lane & 8);
#pragma unroll
  for (int j = 0; j < 4; ++j) x[j] = trstep<HMIR>(x[j], x[j + 4], lane & 4);
#pragma unroll
  for (int j = 0; j < 2; ++j) x[j] = trstep<XOR2>(x[j], x[j + 2], lane & 2);
  const float d = trstep<XOR1>(x[0], x[1], lane & 1);
  return rbf(rbf(d) * 0.08838834764831845f);
}
template <int J> __device__ __forceinline__ v4u pv_diag(u32 phl, const v4u& sel) {
  const u32 t = dppu<BCAST + J>(phl);
  return v4u{__builtin_amdgcn_perm(t, t, sel.x), __builtin_amdgcn_perm(t, t, sel.y), __builtin_amdgcn_perm(t, t, sel.z), __builtin_amdgcn_perm(t, t, sel.w)};
}
#define HOLD(T) "pace%=:\n\ts_memrealtime s[40:41]\n\ts_waitcnt lgkmcnt(0)\n\ts_sub_u32 s40, s40, " T "\n\ts_cmp_ge_i32 s40, 0\n\t" \
                "s_cbranch_scc1 paced%=\n\ts_sleep 1\n\ts_branch pace%=\npaced%=:\n\t"
__device__ __forceinline__ void hold(u32 t) { asm volatile(HOLD("%0") :: "s"(t) : "s40", "s41", "scc"); }
__device__ __forceinline__ void dma4(rsrc_t rs, const v4u& vo, u32 soff, u32 lds) {
  u32 keep;
  asm volatile(
      "s_mov_b32 %0, m0\n\ts_mov_b32 m0, %7\n\ts_nop 0\n\t"
      "buffer_load_dwordx4 %1, %5, %6 offen nt lds\n\tbuffer_load_dwordx4 %2, %5, %6 offen offset:1024 nt lds\n\t"
      "buffer_load_dwordx4 %3, %5, %6 offen offset:2048 nt lds\n\tbuffer_load_dwordx4 %4, %5, %6 offen offset:3072 nt lds\n\t"
      "s_mov_b32 m0, %0"
      : "=&s"(keep) : "v"(vo.x), "v"(vo.y), "v"(vo.z), "v"(vo.w), "s"(rs), "s"(soff), "s"(lds) : "memory");
}
#define LOAD4(A, B, C, D, RS, SO) \
  "buffer_load_dwordx4 %0, " A ", " RS ", " SO " offen nt\n\tbuffer_load_dwordx4 %1, " B ", " RS ", " SO " offen offset:1024 nt\n\t" \
  "buffer_load_dwordx4 %2, " C ", " RS ", " SO " offen offset:2048 nt\n\tbuffer_load_dwordx4 %3, " D ", " RS ", " SO " offen offset:3072 nt\n\t"
__device__ __forceinline__ void load4_first(v4u* d, rsrc_t rs, const v4u& vo, u32 soff) {
  asm volatile(LOAD4("%4", "%5", "%6", "%7", "%8", "%9")
               : "=&v"(d[0]), "=&v"(d[1]), "=&v"(d[2]), "=&v"(d[3]) : "v"(vo.x), "v"(vo.y), "v"(vo.z), "v"(vo.w), "s"(rs), "s"(soff));
}
__device__ __forceinline__ void load4(v4u* d, rsrc_t rs, const v4u& vo, u32 soff) {
  asm volatile(LOAD4("%4", "%5", "%6", "%7", "%8", "%9")
               : "+v"(d[0]), "+v"(d[1]), "+v"(d[2]), "+v"(d[3]) : "v"(vo.x), "v"(vo.y), "v"(vo.z), "v"(vo.w), "s"(rs), "s"(soff));
}
template <int N> __device__ __forceinline__ void landed(v4u* d) {
  asm volatile("s_waitcnt vmcnt(%4)" : "+v"(d[0]), "+v"(d[1]), "+v"(d[2]), "+v"(d[3]) : "i"(N));
}
template <int NL, int ND> __device__ __forceinline__ void step4(v4u* d, v4u* u, rsrc_t rs, const v4u& vo, u32 soff, u32 t, u32 live) {
  asm volatile(
      "s_cmp_eq_u32 %15, 0\n\ts_cbranch_scc1 dead%=\n" HOLD("%14") LOAD4("%8", "%9", "%10", "%11", "%12", "%13")
      "s_waitcnt vmcnt(%16)\n\ts_branch done%=\n"
      "dead%=:\n\ts_waitcnt vmcnt(%17)\n"
      "done%=:"
      : "+v"(d[0]), "+v"(d[1]), "+v"(d[2]), "+v"(d[3]), "+v"(u[0]), "+v"(u[1]), "+v"(u[2]), "+v"(u[3])
      : "v"(vo.x), "v"(vo.y), "v"(vo.z), "v"(vo.w), "s"(rs), "s"(soff), "s"(t), "s"(live), "i"(NL), "i"(ND) : "s40", "s41", "scc");
}

__global__ void __launch_bounds__(512, 2) fused_block(Params p) {
  __shared__ __attribute__((aligned(16))) Lds L_;
  LAS Lds* L = (LAS Lds*)&L_;
  const int lane = threadIdx.x & 63, wave = __builtin_amdgcn_readfirstlane(threadIdx.x >> 6);
  const int r = lane >> 4, c = lane & 15;
  const int xl = blockIdx.x & 7, l = blockIdx.x >> 3;
  const int bh = (l >> 2) * 8 + xl, b = bh >> 5, h = bh & 31, s = l & 3;
  u32 t0 = now();

  if (wave >= 4) {
    const int w = wave - 4;
    const rsrc_t rk = mkrs(p.K + (size_t)bh * (8192 * 128), 8192 * 256), rv = mkrs(p.V + (size_t)bh * (8192 * 128), 8192 * 256);
    const u32 v0 = s * 16384 + w * 1024 + lane * 16;
    const v4u vo = {v0, v0 + 3072, v0 + 2 * 3072, v0 + 3 * 3072};
    v4u a[48];
    auto issue = [&](auto G) {
      constexpr int g = G, d0 = due_of(g), dF = due_of(PAUSE_G) + PAUSE_F + (((g - PAUSE_G) * FAST) >> 4), dR = dF - PAUSE_F + PAUSE_R;
      static_assert(g < 31 + LAG || dF <= d0, "caught up before the loop over V");
      if constexpr (g >= PAUSE_G && dF > d0) hold(t0 + (l < 12 ? dF : (l >= 24 && dR > d0) ? dR : d0));
      else hold(t0 + d0);
      if constexpr (g < 20) asm volatile("s_waitcnt vmcnt(%0)" :: "i"(g < DEPTH_G ? DEPTH - 4 : 28));
      if constexpr (g < 8) dma4(rk, vo, g * 65536, (u32)(uintptr_t)&L->kst[w][4 * g][0]);
      else if constexpr (g < 20) load4_first(a + 4 * slot_of(g), rk, vo, g * 65536);
      else load4(a + 4 * slot_of(g), g < 32 ? rk : rv, vo, (g & 31) * 65536);
      __builtin_amdgcn_sched_barrier(0);
    };
    __builtin_amdgcn_s_barrier();
    Unroll<0, 20>::run(issue);
    lds_wait(&L->cnt[C_Q], 1);
    t0 += __builtin_amdgcn_readfirstlane(L->cnt[C_SHIFT]);
    const v4u q = *(LAS v4u*)&L->qd[4 * c];
    float S[8], x[16];
    auto use = [&](auto G, auto LAST) {
      constexpr int g = G;
      if constexpr (g >= 8) landed<4 * (LAST - g)>(a + 4 * slot_of(g));
#pragma unroll
      for (int k = 0; k < 4; ++k) {
        if constexpr (g < 8) x[4 * (g & 3) + k] = qk(L->kst[w][4 * g + k][lane], q);
        else x[4 * (g & 3) + k] = qk(a[4 * slot_of(g) + k], q);
      }
      if constexpr ((g & 3) == 3) S[g >> 2] = tile_scores(x, lane);
      __builtin_amdgcn_sched_barrier(0);
    };
    Unroll<8, 12>::run([&](auto U) { use(U, std::integral_constant<int, 19>{}); });
    asm volatile("" ::: "memory");
    issue(std::integral_constant<int, 20>{});
    Unroll<0, 4>::run([&](auto U) { use(U, std::integral_constant<int, 20>{}); });
    issue(std::integral_constant<int, 21>{});
    Unroll<4, 8>::run([&](auto U) { use(U, std::integral_constant<int, 21>{}); });
    Unroll<22, 32 + LAG>::run([&](auto G) {
      issue(G);
      Unroll<used_after(G - 1) + 1, used_after(G) + 1>::run([&](auto U) { use(U, G); });
    });
    {
      float m = wave_max(fmaxf(fmaxf(fmaxf(S[0], S[1]), fmaxf(S[2], S[3])), fmaxf(fmaxf(S[4], S[5]), fmaxf(S[6], S[7]))));
      if (lane == 0) L->mw[w] = m;
      lds_arrive(&L->cnt[C_MAX]);
      lds_wait(&L->cnt[C_MAX], 4);
      const v4f m4 = *(LAS v4f*)L->mw;
      const float ml = fmaxf(fmaxf(m4.x, m4.y), fmaxf(m4.z, m4.w)) * 1.4426950408889634f;
      float ls = 0.f;
#pragma unroll
      for (int t = 0; t < 8; ++t) {
        const float pv = __builtin_amdgcn_exp2f(fmaf(S[t], 1.4426950408889634f, -ml));
        ls += pv;
        S[t] = __uint_as_float(pack2(pv, pv - rbf(pv)));
      }
      ls = wave_sum(ls);
      if (lane == 0) L->lw[w] = ls;
      lds_arrive(&L->cnt[C_SUM]);
    }
    v4f acc = {0.f, 0.f, 0.f, 0.f};
    v4u sel;
    {
      const u32 pick = (c & 8) ? 0x0302u : 0x0100u, own = (c & 1) ? (pick << 16) | 0x0c0cu : 0x0c0c0000u | pick;
#pragma unroll
      for (int d = 0; d < 4; ++d) sel[d] = ((c & 7) >> 1) == d ? own : 0x0c0c0c0cu;
    }
#pragma unroll 1
    for (int it = 0; it < 4; ++it) {
      const u32 tb = t0 + ((it * 8 * PACE) >> 4);
      Unroll<0, 8>::run([&](auto J) {
        constexpr int j = J, g = 32 + LAG + j;
        v4u bm[4];
        Unroll<0, 4>::run([&](auto K) { bm[K] = pv_diag<4 * (j & 3) + K>(__float_as_uint(S[j >> 2]), sel); pin(bm[K]); });
        step4<4 * LAG, 4 * (7 - j)>(a + 4 * slot_of(g), a + 4 * slot_of(32 + j), rv, vo, (LAG + j + 8 * it) * 65536, tb + due_of(g), j + LAG < 8 ? 1 : 3 - it);
#pragma unroll
        for (int k = 0; k < 4; ++k)
          acc = __builtin_amdgcn_mfma_f32_16x16x32_bf16(__builtin_bit_cast(bf16x8, a[4 * slot_of(32 + j) + k]), __builtin_bit_cast(bf16x8, bm[k]), acc, 0, 0, 0);
        pin(acc);
        __builtin_amdgcn_sched_barrier(0);
      });
#pragma unroll
      for (int t = 0; t < 6; ++t) S[t] = S[t + 2];
    }
    LAS float* o = &L->op[w][32 * r + 2 * (c & 8) + (c & 7)];
    const float o0 = acc.x + dppf<ROR8>(acc.x), o1 = acc.y + dppf<ROR8>(acc.y), o2 = acc.z + dppf<ROR8>(acc.z), o3 = acc.w + dppf<ROR8>(acc.w);
    o[0] = (c & 8) ? o2 : o0; o[8] = (c & 8) ? o3 : o1;
    wg_barrier();
    return;
  }

  const int w = wave, t = w * 64 + lane;
  const rsrc_t rs = mkrs(p.ws, WS_END), rm = mkrs(p.wmod, 512 * 3072 * 2);
  const bool first = l >= 16 && w >= 2;
  v2u c0 = {0u, 0u}, c1 = c0;
  v4u W[32];
  if (first) {
    const int row = 64 * xl + 32 * (w - 2) + 4 * (lane >> 3);
    c0 = ((const GAS v2u*)p.c)[row >> 2]; c1 = ((const GAS v2u*)p.c)[128 + (row >> 2)];
#pragma unroll
    for (int k = 0; k < 4; ++k) W[k] = ldw(rm, row * 6144 + (lane & 7) * 16, k * 6144 + (l - 16) * 128);
  }
  const u32 epoch = __hip_atomic_load((GAS u32*)(p.ws + WS_EPOCH), __ATOMIC_RELAXED, __HIP_MEMORY_SCOPE_AGENT);
  const int j8 = 2 * s + (w & 1);
  v4u xs = {0u, 0u, 0u, 0u};
  u32 xo = 0;
  if (w < 2) {
    xs = ((const GAS v4u*)p.x)[b * 64 + lane];
    xo = ((const GAS u32*)p.x)[b * 256 + 32 * j8 + 8 * r + (c & 7)];
    const rsrc_t rq = mkrs(p.wq, 512 * 4096 * 2);
#pragma unroll
    for (int k = 0; k < 16; ++k) W[k] = ldw(rq, (64 * j8 + 16 * r) * 8192 + h * 256 + c * 16, k * 8192);
  }
  __builtin_amdgcn_sched_barrier(0);
  if (threadIdx.x < 16) L->cnt[threadIdx.x] = 0;
  wg_barrier();
  __builtin_amdgcn_sched_barrier(0);
  if (w == 3 && l == 12) {
    u64 pc;
    asm volatile("s_getpc_b64 %0" : "=s"(pc));
#pragma unroll
    for (int k = 0; k < CODE_KB; ++k) W[k] = ((const GAS v4u*)(pc & ~127ull))[k * 64 + lane];
#pragma unroll
    for (int k = 0; k < CODE_KB; ++k) pin(W[k]);
  }
  const u32 tag = ((u32)__builtin_amdgcn_readfirstlane(epoch) | 1u) + 2u;
  if (l == 0 && w == 3) publish(rs, lane == 0 ? WS_T + xl * 16 : OOB, t0, t0, tag);
  if (first) {
    float v[16] = {0.f, 0.f, 0.f, 0.f, 0.f, 0.f, 0.f, 0.f, 0.f, 0.f, 0.f, 0.f, 0.f, 0.f, 0.f, 0.f};
    rows2(W[0], W[1], silu2(c0.x), silu2(c1.x), v);
    rows2(W[2], W[3], silu2(c0.y), silu2(c1.y), v);
#pragma unroll
    for (int j = 0; j < 8; ++j) v[j] = sum32(v[j], v[j + 8]);
#pragma unroll
    for (int j = 0; j < 4; ++j) v[j] = sum16(v[j], v[j + 4]);
    L->red1[w][lane] = v2f{trstep<ROR8>(v[0], v[2], lane & 8), trstep<ROR8>(v[1], v[3], lane & 8)};
    if (last_of(&L->cnt[C_MP], 2, lane)) {
      const int src = (lane & 32) + (lane & 3) * 8 + ((lane >> 2) & 7);
      publish(rs, WS_MP + ((l - 16) * 8 + xl) * 1024 + lane * 16, L->red1[2][src] + L->red1[3][src], tag);
    }
  }
  if (w < 2) {
    v2f mean, rstd;
    {
      v2f e[8];
#pragma unroll
      for (int k = 0; k < 4; ++k) { e[2 * k] = v2f{bflo(xs[k]), bflo(xs[k])}; e[2 * k + 1] = v2f{bfhi(xs[k]), bfhi(xs[k])}; }
      ln_stats(e, lane, mean, rstd);
      pin(mean); pin(rstd);
    }
#pragma unroll
    for (int k = 0; k < 8; ++k) { interleave(W[2 * k], W[2 * k + 1]); pin(W[2 * k]); pin(W[2 * k + 1]); }
    u32 off[8];
#pragma unroll
    for (int k = 0; k < 8; ++k) off[k] = WS_MP + (((c & 8) + j8) * 8 + k) * 1024 + b * 512 + (8 * r + (c & 7)) * 16;
    v4u val[8];
    hold(t0 + MOD_T);
    gather<8, 2>(rs, off, val, tag, t0);
    v2f m = payload(val[0]);
#pragma unroll
    for (int k = 1; k < 8; ++k) m += payload(val[k]);
    const u32 own = pack2(m.x, m.y), oth = dppu<ROR8>(own);
    const v2f hv = modulate(unpack2(xo), mean, rstd, rbf2(unpack2((c & 8) ? own : oth) + 1.f), unpack2((c & 8) ? oth : own));
    float u[4];
    gemv<8>(W, pack2(hv.x, hv.y), u);
    L->red1[w][lane] = v2f{sum16(u[0], u[2]), sum16(u[1], u[3])};
    if (last_of(&L->cnt[C_QP], 2, lane)) {
      publish(rs, WS_QP + (bh * 4 + s) * 1024 + lane * 16, L->red1[0][lane] + L->red1[1][lane], tag);
      lds_arrive(&L->cnt[C_QPUB]);
    }
  } else if (w == 2) {
    const u32 o0 = WS_QP + bh * 4096 + lane * 16, off[5] = {o0, o0 + 1024, o0 + 2048, o0 + 3072, WS_T + (lane & 7) * 16};
    v4u val[5];
    lds_wait(&L->cnt[C_QPUB], 1);
    __builtin_amdgcn_s_sleep(D_Q);
    gather<5, 2>(rs, off, val, tag, t0);
    int d = (int)(val[4].x - t0);
    d = min(d, (int)dppu<XOR1>(d)); d = min(d, (int)dppu<XOR2>(d)); d = min(d, (int)dppu<HMIR>(d));
    L->cnt[C_SHIFT] = max(min(d, 0), -200);
    const v2f qv = (payload(val[0]) + payload(val[1])) + (payload(val[2]) + payload(val[3]));
    L->qd[4 * c + r] = pack2(qv.x, qv.y);
    lds_arrive(&L->cnt[C_Q]);
  }

  const int fstart = (43 * xl + 3) >> 2, fg = fstart + l;
  const bool ffn = fg < (43 * (xl + 1) + 3) >> 2;
  v4u F[8];
  v4u W2[4];
#pragma unroll
  for (int k = 0; k < 8; ++k) F[k] = v4u{0u, 0u, 0u, 0u};
#pragma unroll
  for (int k = 0; k < 4; ++k) W2[k] = v4u{0u, 0u, 0u, 0u};
  if (l >= 24) {
    const int rg = xl * 8 + l - 24, row = 128 * w + 8 * (lane >> 2);
    hold(t0 + STRIP_T);
    const v4u ca = ((const GAS v4u*)p.c)[row >> 3], cb = ((const GAS v4u*)p.c)[64 + (row >> 3)];
#pragma unroll
    for (int k = 0; k < 8; ++k) W[k] = ldw(rm, row * 6144 + (lane & 3) * 16, k * 6144 + 2048 + rg * 64);
    float v[16] = {0.f, 0.f, 0.f, 0.f, 0.f, 0.f, 0.f, 0.f, 0.f, 0.f, 0.f, 0.f, 0.f, 0.f, 0.f, 0.f};
#pragma unroll
    for (int k = 0; k < 4; ++k) rows2(W[2 * k], W[2 * k + 1], silu2(ca[k]), silu2(cb[k]), v);
#pragma unroll
    for (int j = 0; j < 8; ++j) v[j] = sum32(v[j], v[j + 8]);
#pragma unroll
    for (int j = 0; j < 4; ++j) { v[j] = sum16(v[j], v[j + 4]); v[j] += dppf<ROR8>(v[j]); v[j] += dppf<ROR4>(v[j]); }
    if ((lane & 12) == 0) L->red1b[w][4 * r + (lane & 3)] = v4f{v[0], v[1], v[2], v[3]};
    if (last_of(&L->cnt[C_MOD], 4, lane)) {
      const int src = (c & 8) + (c & 1) * 4 + ((c >> 1) & 3);
      const v4f a = (L->red1b[0][src] + L->red1b[1][src]) + (L->red1b[2][src] + L->red1b[3][src]);
      publish(rs, lane < 16 ? WS_MOD + (c >> 3) * 8192 + (8 * rg + (c & 7)) * 16 : OOB, pack2(a.x, a.y), pack2(a.z, a.w), tag);
    }
  } else if (ffn) {
    const rsrc_t r1 = mkrs(p.w1, 512 * 1376 * 2), r3 = mkrs(p.w3, 512 * 1376 * 2), r2 = mkrs(p.w2, 1376 * 512 * 2);
    hold(t0 + STRIP_T);
#pragma unroll
    for (int k = 0; k < 8; ++k) F[k] = ldw(k < 4 ? r1 : r3, (128 * w + (lane >> 1)) * 2752 + (lane & 1) * 16, (k & 3) * 32 * 2752 + fg * 32);
#pragma unroll
    for (int k = 0; k < 4; ++k) W2[k] = ldw(r2, r * 4096 + w * 256 + c * 16, (16 * fg + k) * 1024);
    LAS v4s* tr = (LAS v4s*)&L->stage[w][0] + (8 * r + (c >> 2)) * 4 + (c & 3);
#pragma unroll
    for (int k = 0; k < 8; ++k) {
      L->stage[w][lane] = F[k];
      asm volatile("" ::: "memory");
      const v2u lo = __builtin_bit_cast(v2u, __builtin_amdgcn_ds_read_tr16_b64_v4i16(tr));
      const v2u hi = __builtin_bit_cast(v2u, __builtin_amdgcn_ds_read_tr16_b64_v4i16(tr + 16));
      F[k] = v4u{lo.x, lo.y, hi.x, hi.y};
      asm volatile("" : "+v"(F[k]) :: "memory");
    }
    interleave(W2[0], W2[1]); interleave(W2[2], W2[3]);
  }
  lds_wait(&L->cnt[C_Q], 1);
  t0 += __builtin_amdgcn_readfirstlane(L->cnt[C_SHIFT]);

  const bool red = l < 4;
  const int j = xl * 4 + (l & 3), rp = 64 * (j >> 3) + 16 * (j & 3) + 4 * (lane & 3) + 2 * ((j >> 2) & 1) + ((lane >> 2) & 1);
  const int tp = w * 64 + 16 * ((lane >> 3) & 3) + 4 * (lane & 3) + 2 * (lane >> 5) + ((lane >> 2) & 1);
  v2f sc0 = {0.f, 0.f}, sc1 = sc0, sh0 = sc0, sh1 = sc0;
  float xr = 0.f, gar = 0.f, gbr = 0.f, mloc = 0.f;
  {
    const rsrc_t ro = mkrs(p.wo, 4096 * 512 * 2);
    Unroll<0, 16>::run([&](auto K) {
      hold(t0 + WO_T + K * WO_DT);
      W[2 * K] = ldw(ro, (h * 128 + 2 * r) * 1024 + w * 256 + c * 16, K * 8192);
      W[2 * K + 1] = ldw(ro, (h * 128 + 2 * r) * 1024 + w * 256 + c * 16, K * 8192 + 1024);
      __builtin_amdgcn_sched_barrier(0);
      if constexpr (K == MOD_K) {
        if (ffn) {
          const u32 modb = WS_MOD + (1 + (lane >> 5)) * 2048 + (32 * w + (lane & 31)) * 16;
          const u32 base = WS_MOD + (r >> 1) * 8192 + (rp >> 1) * 16;
          const u32 off[4] = {modb, modb + 8192, (red && w == 0) ? base : OOB, (red && w == 0) ? base + 3 * 2048 : OOB};
          v4u val[4];
          const u32 xv = ((const GAS u32*)p.x)[(r >> 1) * 256 + rp];
          gather<4, 16, 2>(rs, off, val, tag, t0);
          const int src = (tp & 62) * 2;
          auto fetch = [&](int from, const v4u& u) {
            u32 lo = __builtin_amdgcn_ds_bpermute(from, u.x), hi = __builtin_amdgcn_ds_bpermute(from, u.z);
            pin(lo); pin(hi);
            return (tp & 1) ? hi : lo;
          };
          const u32 h0 = fetch(src, val[0]), h1 = fetch(src, val[1]), g0 = fetch(src + 128, val[0]), g1 = fetch(src + 128, val[1]);
          sc0 = rbf2(v2f{bflo(g0), bflo(g1)} + 1.f); sc1 = rbf2(v2f{bfhi(g0), bfhi(g1)} + 1.f);
          sh0 = v2f{bflo(h0), bflo(h1)}; sh1 = v2f{bfhi(h0), bfhi(h1)};
          const u32 ga = (rp & 1) ? val[2].z : val[2].x, gb = (rp & 1) ? val[3].z : val[3].x;
          xr = (r & 1) ? bfhi(xv) : bflo(xv); gar = (r & 1) ? bfhi(ga) : bflo(ga); gbr = (r & 1) ? bfhi(gb) : bflo(gb);
        }
        __builtin_amdgcn_sched_barrier(0);
      }
      if constexpr (K == ML_K) {
        lds_wait(&L->cnt[C_SUM], 4);
        const v4f m4 = *(LAS v4f*)L->mw, l4 = *(LAS v4f*)L->lw;
        mloc = fmaxf(fmaxf(m4.x, m4.y), fmaxf(m4.z, m4.w));
        publish(rs, t == 0 ? WS_ML + (bh * 4 + s) * 16 : OOB, v2f{mloc, (l4.x + l4.y) + (l4.z + l4.w)}, tag);
        __builtin_amdgcn_sched_barrier(0);
      }
    });
  }
#pragma unroll
  for (int k = 0; k < 16; ++k) { interleave(W[2 * k], W[2 * k + 1]); pin(W[2 * k]); pin(W[2 * k + 1]); }
  float scale;
  {
    const u32 off[1] = {WS_ML + (bh * 4 + (lane & 3)) * 16};
    v4u val[1];
    gather<1, 16>(rs, off, val, tag, t0);
    const v2f ml = payload(val[0]);
    float M = fmaxf(ml.x, dppf<XOR1>(ml.x)); M = fmaxf(M, dppf<XOR2>(M));
    float Ls = ml.y * __builtin_amdgcn_exp2f((ml.x - M) * 1.4426950408889634f);
    Ls += dppf<XOR1>(Ls); Ls += dppf<XOR2>(Ls);
    scale = __builtin_amdgcn_exp2f((mloc - M) * 1.4426950408889634f) / Ls;
  }

  u32 o_y = (r & 1) ? OOB : WS_Y + ((8 * w + 2 * r + (c >> 2)) * 256 + bh * 4 + s) * 64 + (c & 3) * 16;
  u32 o_yg = WS_Y + j * 16384 + w * 4096 + lane * 16;
  u32 o_x = (lane >> 3) < REP_X1 ? WS_X1 + (lane >> 3) * 4096 + j * 128 + (lane & 7) * 16 : OOB;
  u32 o_xg[4];
#pragma unroll
  for (int k = 0; k < 4; ++k) { o_xg[k] = WS_X1 + (xl & (REP_X1 - 1)) * 4096 + lane * 16 + ((w + k) & 3) * 1024; pin(o_xg[k]); }
  u32 o_o = WS_OP + ((8 * w + 4 * (r >> 1) + (c >> 2)) * 96 + fg) * 128 + (r & 1) * 64 + (c & 3) * 16;
  u32 o_og[3];
  o_og[0] = WS_OP + j * 12288 + w * 3072 + lane * 16;
  o_og[1] = (w < 3 || (lane >> 3) < NFFN - 80) ? o_og[0] + 1024 : OOB;
  o_og[2] = w < 3 ? o_og[0] + 2048 : OOB;
  u32 o_e = (j == 0 && lane == 0) ? WS_EPOCH : OOB;
  u32 o_out = (lane & 8) == 0 ? ((r >> 1) * 512 + 2 * rp + (r & 1)) * 2 : OOB;
  const rsrc_t rout = mkrs(p.out, 2048);
  LAS v2f* a_o = (LAS v2f*)&L->op[0][8 * c + 2 * r];
  LAS float* a_rw = &L->ry[w][4 * r + (lane & 3)];
  LAS float* a_rr = &L->ry[r & 2][2 * (lane & 4) + 4 * (r & 1) + (lane & 3)];
  LAS u32* a_hw = &L->h2[0][tp];
  LAS v4u* a_hr = (LAS v4u*)&L->h2[c & 1][64 * w + 4 * r];
  LAS float* a_gw = &L->red2[lane][w];
  LAS v4f* a_gr = (LAS v4f*)L->red2[(c & 4) * 4 + 4 * r + (c & 3)];
  LAS float* a_ow = &L->ro[w][8 * r + (lane & 7)];
  LAS float* a_or = &L->ro[0][8 * r + (lane & 7)];
  pin(o_y); pin(o_yg); pin(o_x); pin(o_o); pin(o_og[0]); pin(o_og[1]); pin(o_og[2]); pin(o_e); pin(o_out);
  pin(scale); pin(sc0); pin(sc1); pin(sh0); pin(sh1); pin(xr); pin(gar); pin(gbr);
  pin(a_o); pin(a_rw); pin(a_rr); pin(a_hw); pin(a_hr); pin(a_gw); pin(a_gr); pin(a_ow); pin(a_or);

  wg_barrier();
  {
    const v2f o = ((a_o[0] + a_o[64]) + (a_o[128] + a_o[192])) * scale;
    float u[4];
    gemv<16>(W, pack2(o.x, o.y), u);
    publish(rs, o_y, pack2(sum16(u[0], u[0]), sum16(u[1], u[1])), pack2(sum16(u[2], u[2]), sum16(u[3], u[3])), tag);
  }
  if (ffn) {
    float x1r = 0.f;
    {
      const u32 off[4] = {o_yg, o_yg + 1024, o_yg + 2048, o_yg + 3072};
      v4u val[4];
      __builtin_amdgcn_s_sleep(D_Y);
      gather<4, 0>(rs, off, val, tag, t0);
      const v2f lo = (unpack2(val[0].x) + unpack2(val[1].x)) + (unpack2(val[2].x) + unpack2(val[3].x));
      const v2f hi = (unpack2(val[0].z) + unpack2(val[1].z)) + (unpack2(val[2].z) + unpack2(val[3].z));
      float a = sum16(sum32(lo.x, hi.x), sum32(lo.y, hi.y));
      a += dppf<ROR8>(a); a += dppf<ROR4>(a);
      a_rw[0] = a;
      wg_barrier();
      if (red && w == 0) {
        x1r = rbf(xr + rbf(gar * rbf(a_rr[0] + a_rr[16])));
        const auto e = __builtin_amdgcn_permlane16_swap(__float_as_uint(x1r), __float_as_uint(x1r), false, false);
        const u32 pr = (e[0] >> 16) | e[1];
        const auto bb = __builtin_amdgcn_permlane32_swap(pr, pr, false, false);
        publish(rs, o_x, bb[0], bb[1], tag);
        __builtin_amdgcn_raw_buffer_store_b32(tag, rs, o_e, 0, AUX_SC1);
        __builtin_amdgcn_s_sleep(D_XR);
      } else __builtin_amdgcn_s_sleep(D_X);
    }
    v4u val[4];
    gather<4, 0>(rs, o_xg, val, tag, t0);
    v2f e[8], mean, rstd;
#pragma unroll
    for (int k = 0; k < 4; ++k) { e[2 * k] = v2f{bflo(val[k].x), bflo(val[k].z)}; e[2 * k + 1] = v2f{bfhi(val[k].x), bfhi(val[k].z)}; }
    ln_stats(e, lane, mean, rstd);
    const v2f ha = modulate(e[0], mean, rstd, sc0, sh0), hb = modulate(e[1], mean, rstd, sc1, sh1);
    a_hw[0] = pack2(ha.x, hb.x); a_hw[256] = pack2(ha.y, hb.y);
    asm volatile("" ::: "memory");
    v4u A[4];
#pragma unroll
    for (int k = 0; k < 4; ++k) A[k] = a_hr[4 * k];
    __builtin_amdgcn_sched_barrier(0);
    v4f dg = {0.f, 0.f, 0.f, 0.f}, du = dg;
#pragma unroll
    for (int k = 0; k < 4; ++k) {
      dg = __builtin_amdgcn_mfma_f32_16x16x32_bf16(__builtin_bit_cast(bf16x8, A[k]), __builtin_bit_cast(bf16x8, F[k]), dg, 0, 0, 0);
      du = __builtin_amdgcn_mfma_f32_16x16x32_bf16(__builtin_bit_cast(bf16x8, A[k]), __builtin_bit_cast(bf16x8, F[4 + k]), du, 0, 0, 0);
    }
    const float lo = (lane & 16) ? dg.y : dg.x, hi = (lane & 16) ? du.y : du.x;
    a_gw[0] = (lane & 32) ? hi : lo;
    wg_barrier();
    const v4f g4 = a_gr[0], u4 = a_gr[32];
    const v2f gu = rbf2(v2f{(g4.x + g4.y) + (g4.z + g4.w), (u4.x + u4.y) + (u4.z + u4.w)});
    const float hh = rbf(silu(gu.x)) * gu.y;
    u32 hp = pack2(hh, dppf<XOR1>(hh));
    float o[16] = {0.f, 0.f, 0.f, 0.f, 0.f, 0.f, 0.f, 0.f, 0.f, 0.f, 0.f, 0.f, 0.f, 0.f, 0.f, 0.f};
    dpp_ready(hp);
    dot8<0>(W2[0], W2[1], hp, o); dot8<4>(W2[0], W2[1], hp, o + 8); dot8<2>(W2[2], W2[3], hp, o); dot8<6>(W2[2], W2[3], hp, o + 8);
    dots_done(o); dots_done(o + 8);
#pragma unroll
    for (int k = 0; k < 4; ++k) { o[k] = sum32(o[k], o[k + 4]); o[8 + k] = sum32(o[8 + k], o[12 + k]); }
    publish(rs, o_o, pack2(sum16(o[0], o[2]), sum16(o[1], o[3])), pack2(sum16(o[8], o[10]), sum16(o[9], o[11])), tag);
    if (red) {
      v4u va[3];
      __builtin_amdgcn_s_sleep(D_O);
      gather<3, 0, 1>(rs, o_og, va, tag, t0);
      const v2f b0 = unpack2(va[0].x) + unpack2(va[1].x) + unpack2(va[2].x), b1 = unpack2(va[0].z) + unpack2(va[1].z) + unpack2(va[2].z);
      float a = sum16(sum32(b0.x, b1.x), sum32(b0.y, b1.y));
      a += dppf<ROR8>(a);
      a_ow[0] = a;
      wg_barrier();
      if (w == 0) {
        const float res = x1r + rbf(gbr * rbf((a_or[0] + a_or[32]) + (a_or[64] + a_or[96])));
        __builtin_amdgcn_raw_buffer_store_b16((u16)pack2(res, res), rout, o_out, 0, 0);
      }
    }
  }
}

extern "C" void kernel_launch(void* const* d_in, const int* in_sizes, int n_in, void* d_out, int out_size, void* d_ws, size_t ws_size, hipStream_t stream) {
  Params p;
  p.x = (const u16*)d_in[0]; p.K = (const u16*)d_in[1]; p.V = (const u16*)d_in[2]; p.wq = (const u16*)d_in[3]; p.wo = (const u16*)d_in[4];
  p.w1 = (const u16*)d_in[6]; p.w3 = (const u16*)d_in[7]; p.w2 = (const u16*)d_in[8]; p.c = (const u16*)d_in[9]; p.wmod = (const u16*)d_in[10];
  p.out = (u16*)d_out; p.ws = (unsigned char*)d_ws;
  hipLaunchKernelGGL(fused_block, dim3(256), dim3(512), 0, stream, p);
}
```

```cpp
#include <hip/hip_runtime.h>
#include <stdint.h>
#include <type_traits>

typedef unsigned short u16;
typedef unsigned int u32;
typedef unsigned long long u64;
typedef u32 v4u __attribute__((ext_vector_type(4)));
typedef u32 v2u __attribute__((ext_vector_type(2)));
typedef float v4f __attribute__((ext_vector_type(4)));
typedef float v2f __attribute__((ext_vector_type(2)));
typedef __bf16 bf16x2 __attribute__((ext_vector_type(2)));
typedef __bf16 bf16x8 __attribute__((ext_vector_type(8)));
typedef short v4s __attribute__((ext_vector_type(4)));
typedef __amdgpu_buffer_rsrc_t rsrc_t;
#define GAS __attribute__((address_space(1)))
#define LAS __attribute__((address_space(3)))

#define PACE16 945
constexpr int KSTART = 100;
constexpr int PACE = PACE16;
constexpr int DEPTH = 8, DEPTH_G = 10;
constexpr int LAG = 7;
#define PF_ 370
#define PR_ 200
#define PP_ 0
#define FASTF 810
#define D_H 10
#define WOT 0
constexpr int PAUSE_G = 12, PAUSE_F = PF_, PAUSE_R = PR_, FAST = FASTF;
constexpr int MOD_T = 320, STRIP_T = 830, WO_T = 1900, WO_DT = 70, MOD_K = 1, ML_K = 6;
constexpr int D_Q = 30, D_Y = 12, D_X = 20, D_XR = 17, D_O = 18;
constexpr u32 SPIN_TICKS = 400000;
constexpr int CODE_KB = 26;
constexpr int due_of(int g) { return KSTART + ((g * PACE) >> 4); }
constexpr int slot_of(int g) { return g < 20 ? g - 8 : (g - 20) & 7; }
constexpr int used_after(int g) { return (g <= 21 || g - LAG < 11) ? 11 : g - LAG; }

constexpr int NFFN = 86, REP_X1 = 4;
constexpr u32 WS_EPOCH = 0;
constexpr u32 WS_MP = 256;
constexpr u32 WS_MOD = WS_MP + 16 * 8 * 1024;
constexpr u32 WS_ML = WS_MOD + 2 * 8192;
constexpr u32 WS_T = WS_ML + 4096;
constexpr u32 WS_X1 = WS_T + 128;
constexpr u32 WS_QP = WS_X1 + REP_X1 * 4096;
constexpr u32 WS_O = WS_QP + 256 * 1024;
constexpr u32 WS_Y = WS_O + 256 * 1024;
constexpr u32 WS_OP = WS_Y + 32 * 256 * 64;
constexpr u32 WS_END = WS_OP + 32 * 96 * 128;
constexpr u32 OOB = 0x7ffffff0u;
constexpr int AUX_SC1 = 16, AUX_NT = 2;
#define STAMP(i) do {} while (0)
#define STAMPS_OUT() do {} while (0)

struct Params {
  const u16 *x, *K, *V, *wq, *wo, *w1, *w3, *w2, *c, *wmod;
  u16* out;
  unsigned char* ws;
};

enum { C_MP, C_Q, C_MAX, C_SUM, C_MOD, C_QP, C_QPUB, C_SHIFT };
struct Lds {
  u32 cnt[16];
  v2f red1[4][64];
  v4f red1b[4][16];
  u32 qd[64];
  float mw[4], lw[4];
  float op[4][128];
  float red2[64][4];
  u32 h2[2][256];
  float ry[4][16], ro[4][32];
  v4u stage[4][64];
  v4u kst[4][32][64];
};

__device__ __forceinline__ float bflo(u32 v) { return __uint_as_float(v << 16); }
__device__ __forceinline__ float bfhi(u32 v) { return __uint_as_float(v & 0xffff0000u); }
__device__ __forceinline__ v2f unpack2(u32 v) { return v2f{bflo(v), bfhi(v)}; }
__device__ __forceinline__ u32 pack2(float a, float b) { bf16x2 r = {(__bf16)a, (__bf16)b}; return __builtin_bit_cast(u32, r); }
__device__ __forceinline__ float rbf(float a) { return bflo(pack2(a, a)); }
__device__ __forceinline__ v2f rbf2(v2f v) { u32 r = pack2(v.x, v.y); asm("" : "+v"(r)); return unpack2(r); }
__device__ __forceinline__ float dot2(u32 a, u32 b, float acc) {
  return __builtin_amdgcn_fdot2_f32_bf16(__builtin_bit_cast(bf16x2, a), __builtin_bit_cast(bf16x2, b), acc, false);
}
__device__ __forceinline__ float silu(float v) { return v * __builtin_amdgcn_rcpf(1.f + __expf(-v)); }
__device__ __forceinline__ u32 silu2(u32 v) { return pack2(silu(bflo(v)), silu(bfhi(v))); }
template <int CTRL> __device__ __forceinline__ u32 dppu(u32 x) { return (u32)__builtin_amdgcn_mov_dpp((int)x, CTRL, 0xf, 0xf, true); }
template <int CTRL> __device__ __forceinline__ float dppf(float x) { return __uint_as_float(dppu<CTRL>(__float_as_uint(x))); }
constexpr int XOR1 = 0xB1, XOR2 = 0x4E, HMIR = 0x141, ROR4 = 0x124, ROR8 = 0x128, BCAST = 0x150;
__device__ __forceinline__ float sum32(float a, float b) {
  auto s = __builtin_amdgcn_permlane32_swap(__float_as_uint(a), __float_as_uint(b), false, false);
  return __uint_as_float(s[0]) + __uint_as_float(s[1]);
}
__device__ __forceinline__ float sum16(float a, float b) {
  auto s = __builtin_amdgcn_permlane16_swap(__float_as_uint(a), __float_as_uint(b), false, false);
  return __uint_as_float(s[0]) + __uint_as_float(s[1]);
}
template <int CTRL> __device__ __forceinline__ float trstep(float lo, float hi, bool bit) {
  const float a = lo + dppf<CTRL>(lo), b = hi + dppf<CTRL>(hi);
  return bit ? b : a;
}
__device__ __forceinline__ float wave_sum(float x) {
  x = sum32(x, x); x = sum16(x, x);
  x += dppf<ROR8>(x); x += dppf<HMIR>(x); x += dppf<XOR2>(x); x += dppf<XOR1>(x);
  return x;
}
__device__ __forceinline__ float wave_max(float x) {
  x = fmaxf(x, dppf<XOR1>(x)); x = fmaxf(x, dppf<XOR2>(x)); x = fmaxf(x, dppf<HMIR>(x)); x = fmaxf(x, dppf<ROR8>(x));
  auto s = __builtin_amdgcn_permlane16_swap(__float_as_uint(x), __float_as_uint(x), false, false);
  x = fmaxf(__uint_as_float(s[0]), __uint_as_float(s[1]));
  auto t = __builtin_amdgcn_permlane32_swap(__float_as_uint(x), __float_as_uint(x), false, false);
  return fmaxf(__uint_as_float(t[0]), __uint_as_float(t[1]));
}
__device__ __forceinline__ rsrc_t mkrs(const void* p, u32 bytes) { return __builtin_amdgcn_make_buffer_rsrc((void*)p, 0, bytes, 0x00020000); }
__device__ __forceinline__ v4u ldw(rsrc_t rs, u32 voff, u32 soff) { return __builtin_amdgcn_raw_buffer_load_b128(rs, voff, soff, AUX_NT); }
__device__ __forceinline__ u32 now() { return (u32)__builtin_amdgcn_s_memrealtime(); }
template <class T> __device__ __forceinline__ void pin(T& v) { asm volatile("" : "+v"(v)); }
template <int I, int E> struct Unroll {
  template <class F> static __device__ __forceinline__ void run(F&& f) {
    if constexpr (I < E) { f(std::integral_constant<int, I>{}); Unroll<I + 1, E>::run(f); }
  }
};

__device__ __forceinline__ void lds_arrive(LAS u32* c) {
  asm volatile("s_mov_b64 exec, 1\n\tds_add_u32 %0, %1\n\ts_mov_b64 exec, -1" :: "v"((u32)(uintptr_t)c), "v"(1u) : "memory");
}
__device__ __forceinline__ void lds_wait(LAS u32* c, u32 n) {
  while ((u32)__builtin_amdgcn_readfirstlane(__hip_atomic_load(c, __ATOMIC_RELAXED, __HIP_MEMORY_SCOPE_WORKGROUP)) < n) __builtin_amdgcn_s_sleep(1);
  asm volatile("" ::: "memory");
}
__device__ __forceinline__ bool last_of(LAS u32* c, u32 n, int lane) {
  asm volatile("" ::: "memory");
  u32 old = 0;
  if (lane == 0) old = __hip_atomic_fetch_add(c, 1u, __ATOMIC_RELAXED, __HIP_MEMORY_SCOPE_WORKGROUP);
  const bool last = (u32)__builtin_amdgcn_readfirstlane(old) == n - 1u;
  asm volatile("" ::: "memory");
  return last;
}
__device__ __forceinline__ void wg_barrier() { asm volatile("s_waitcnt lgkmcnt(0)\n\ts_barrier" ::: "memory"); }

__device__ __forceinline__ void publish(rsrc_t rs, u32 off, u32 a, u32 b, u32 tag) {
  __builtin_amdgcn_raw_buffer_store_b128(v4u{a, tag, b, tag}, rs, off, 0, AUX_SC1);
}
__device__ __forceinline__ void publish(rsrc_t rs, u32 off, v2f v, u32 tag) { publish(rs, off, __float_as_uint(v.x), __float_as_uint(v.y), tag); }
template <int N, int SLEEP, int NFULL = N> __device__ __forceinline__ void gather(rsrc_t rs, const u32 (&off)[N], v4u (&val)[N], u32 tag, u32 t0) {
  for (;;) {
#pragma unroll
    for (int k = 0; k < N; ++k) val[k] = __builtin_amdgcn_raw_buffer_load_b128(rs, off[k], 0, AUX_SC1);
    u32 bad = 0u;
#pragma unroll
    for (int k = 0; k < N; ++k) {
      const u32 m = (val[k].y ^ tag) | (val[k].w ^ tag);
      bad |= (k >= NFULL && off[k] == OOB) ? 0u : m;
    }
    asm("" : "+v"(bad));
    if (__builtin_expect(!__any(bad != 0u), 1) || now() - t0 > SPIN_TICKS) break;
    if (SLEEP) __builtin_amdgcn_s_sleep(SLEEP);
  }
}
__device__ __forceinline__ v2f payload(const v4u& u) { return v2f{__uint_as_float(u.x), __uint_as_float(u.z)}; }

__device__ __forceinline__ void interleave(v4u& a, v4u& b) {
  const v4u lo = {__builtin_amdgcn_perm(b.x, a.x, 0x05040100u), __builtin_amdgcn_perm(b.x, a.x, 0x07060302u),
                  __builtin_amdgcn_perm(b.y, a.y, 0x05040100u), __builtin_amdgcn_perm(b.y, a.y, 0x07060302u)};
  const v4u hi = {__builtin_amdgcn_perm(b.z, a.z, 0x05040100u), __builtin_amdgcn_perm(b.z, a.z, 0x07060302u),
                  __builtin_amdgcn_perm(b.w, a.w, 0x05040100u), __builtin_amdgcn_perm(b.w, a.w, 0x07060302u)};
  a = lo; b = hi;
}
__device__ __forceinline__ void rows2(v4u a, v4u b, u32 s0, u32 s1, float (&v)[16]) {
  interleave(a, b);
#pragma unroll
  for (int j = 0; j < 4; ++j) {
    v[j] = dot2(a[j], s0, v[j]); v[4 + j] = dot2(b[j], s0, v[4 + j]);
    v[8 + j] = dot2(a[j], s1, v[8 + j]); v[12 + j] = dot2(b[j], s1, v[12 + j]);
  }
}
template <int LANE> __device__ __forceinline__ void dot8(const v4u& wa, const v4u& wb, u32 vp, float* a) {
#define DOT(i, j) "v_dot2c_f32_bf16_dpp %" #i ", %8, %" #j " row_newbcast:%c17 row_mask:0xf bank_mask:0xf\n\t"
  asm(DOT(0, 9) DOT(1, 10) DOT(2, 11) DOT(3, 12) DOT(4, 13) DOT(5, 14) DOT(6, 15) DOT(7, 16)
      : "+v"(a[0]), "+v"(a[1]), "+v"(a[2]), "+v"(a[3]), "+v"(a[4]), "+v"(a[5]), "+v"(a[6]), "+v"(a[7])
      : "v"(vp), "v"(wa.x), "v"(wa.y), "v"(wa.z), "v"(wa.w), "v"(wb.x), "v"(wb.y), "v"(wb.z), "v"(wb.w), "i"(LANE));
#undef DOT
}
__device__ __forceinline__ void dpp_ready(u32& vp) { asm("s_nop 1" : "+v"(vp)); }
__device__ __forceinline__ void dots_done(float* a) {
  asm("s_nop 2" : "+v"(a[0]), "+v"(a[1]), "+v"(a[2]), "+v"(a[3]), "+v"(a[4]), "+v"(a[5]), "+v"(a[6]), "+v"(a[7]));
}
template <int NK> __device__ __forceinline__ void gemv(const v4u (&W)[32], u32 vp, float (&u)[4]) {
  float a[8] = {0.f, 0.f, 0.f, 0.f, 0.f, 0.f, 0.f, 0.f};
  dpp_ready(vp);
  Unroll<0, NK>::run([&](auto K) { dot8<K>(W[2 * K], W[2 * K + 1], vp, a); });
  dots_done(a);
#pragma unroll
  for (int j = 0; j < 4; ++j) u[j] = sum32(a[j], a[j + 4]);
}

__device__ __forceinline__ void ln_stats(const v2f (&e)[8], int lane, v2f& mean, v2f& rstd) {
  const v2f piv = {__uint_as_float(__builtin_amdgcn_readlane(__float_as_uint(e[0].x), 0)), __uint_as_float(__builtin_amdgcn_readlane(__float_as_uint(e[0].y), 0))};
  v2f d[8];
#pragma unroll
  for (int j = 0; j < 8; ++j) d[j] = e[j] - piv;
  const v2f s = ((d[0] + d[1]) + (d[2] + d[3])) + ((d[4] + d[5]) + (d[6] + d[7]));
  v2f qa = d[0] * d[0], qb = d[1] * d[1];
#pragma unroll
  for (int j = 2; j < 8; j += 2) { qa += d[j] * d[j]; qb += d[j + 1] * d[j + 1]; }
  qa += qb;
  float a = sum16(sum32(s.x, s.y), sum32(qa.x, qa.y));
  a += dppf<ROR8>(a); a += dppf<HMIR>(a); a += dppf<XOR2>(a); a += dppf<XOR1>(a);
  const auto t = __builtin_amdgcn_permlane16_swap(__float_as_uint(a), __float_as_uint(a), false, false);
  const float md = __uint_as_float(t[0]) * (1.f / 512.f), var = fmaxf(fmaf(-md, md, __uint_as_float(t[1]) * (1.f / 512.f)), 0.f);
  const v2f mv = rbf2(v2f{md + (lane < 32 ? piv.x : piv.y), var});
  const float rs = __builtin_amdgcn_rcpf(rbf(__builtin_amdgcn_sqrtf(rbf(mv.y + 1e-5f))));
  const auto m2 = __builtin_amdgcn_permlane32_swap(__float_as_uint(mv.x), __float_as_uint(mv.x), false, false);
  const auto r2 = __builtin_amdgcn_permlane32_swap(__float_as_uint(rs), __float_as_uint(rs), false, false);
  mean = v2f{__uint_as_float(m2[0]), __uint_as_float(m2[1])};
  rstd = v2f{__uint_as_float(r2[0]), __uint_as_float(r2[1])};
}
__device__ __forceinline__ v2f modulate(v2f x, v2f mean, v2f rstd, v2f scale1, v2f shift) { return rbf2(rbf2(rbf2(x - mean) * rstd) * scale1) + shift; }

__device__ __forceinline__ float qk(const v4u k, const v4u q) { return dot2(k.w, q.w, dot2(k.z, q.z, dot2(k.y, q.y, dot2(k.x, q.x, 0.f)))); }
__device__ __forceinline__ float tile_scores(float (&x)[16], int lane) {
#pragma unroll
  for (int j = 0; j < 8; ++j) x[j] = trstep<ROR8>(x[j], x[j + 8], lane & 8);
#pragma unroll
  for (int j = 0; j < 4; ++j) x[j] = trstep<HMIR>(x[j], x[j + 4], lane & 4);
#pragma unroll
  for (int j = 0; j < 2; ++j) x[j] = trstep<XOR2>(x[j], x[j + 2], lane & 2);
  const float d = trstep<XOR1>(x[0], x[1], lane & 1);
  return rbf(rbf(d) * 0.08838834764831845f);
}
template <int J> __device__ __forceinline__ v4u pv_diag(u32 phl, const v4u& sel) {
  const u32 t = dppu<BCAST + J>(phl);
  return v4u{__builtin_amdgcn_perm(t, t, sel.x), __builtin_amdgcn_perm(t, t, sel.y), __builtin_amdgcn_perm(t, t, sel.z), __builtin_amdgcn_perm(t, t, sel.w)};
}
#define HOLD(T) "pace%=:\n\ts_memrealtime s[40:41]\n\ts_waitcnt lgkmcnt(0)\n\ts_sub_u32 s40, s40, " T "\n\ts_cmp_ge_i32 s40, 0\n\t" \
                "s_cbranch_scc1 paced%=\n\ts_sleep 1\n\ts_branch pace%=\npaced%=:\n\t"
__device__ __forceinline__ void hold(u32 t) { asm volatile(HOLD("%0") :: "s"(t) : "s40", "s41", "scc"); }
__device__ __forceinline__ void dma4(rsrc_t rs, const v4u& vo, u32 soff, u32 lds) {
  u32 keep;
  asm volatile(
      "s_mov_b32 %0, m0\n\ts_mov_b32 m0, %7\n\ts_nop 0\n\t"
      "buffer_load_dwordx4 %1, %5, %6 offen nt lds\n\tbuffer_load_dwordx4 %2, %5, %6 offen offset:1024 nt lds\n\t"
      "buffer_load_dwordx4 %3, %5, %6 offen offset:2048 nt lds\n\tbuffer_load_dwordx4 %4, %5, %6 offen offset:3072 nt lds\n\t"
      "s_mov_b32 m0, %0"
      : "=&s"(keep) : "v"(vo.x), "v"(vo.y), "v"(vo.z), "v"(vo.w), "s"(rs), "s"(soff), "s"(lds) : "memory");
}
#define LOAD4(A, B, C, D, RS, SO) \
  "buffer_load_dwordx4 %0, " A ", " RS ", " SO " offen nt\n\tbuffer_load_dwordx4 %1, " B ", " RS ", " SO " offen offset:1024 nt\n\t" \
  "buffer_load_dwordx4 %2, " C ", " RS ", " SO " offen offset:2048 nt\n\tbuffer_load_dwordx4 %3, " D ", " RS ", " SO " offen offset:3072 nt\n\t"
__device__ __forceinline__ void load4_first(v4u* d, rsrc_t rs, const v4u& vo, u32 soff) {
  asm volatile(LOAD4("%4", "%5", "%6", "%7", "%8", "%9")
               : "=&v"(d[0]), "=&v"(d[1]), "=&v"(d[2]), "=&v"(d[3]) : "v"(vo.x), "v"(vo.y), "v"(vo.z), "v"(vo.w), "s"(rs), "s"(soff));
}
__device__ __forceinline__ void load4(v4u* d, rsrc_t rs, const v4u& vo, u32 soff) {
  asm volatile(LOAD4("%4", "%5", "%6", "%7", "%8", "%9")
               : "+v"(d[0]), "+v"(d[1]), "+v"(d[2]), "+v"(d[3]) : "v"(vo.x), "v"(vo.y), "v"(vo.z), "v"(vo.w), "s"(rs), "s"(soff));
}
template <int N> __device__ __forceinline__ void landed(v4u* d) {
  asm volatile("s_waitcnt vmcnt(%4)" : "+v"(d[0]), "+v"(d[1]), "+v"(d[2]), "+v"(d[3]) : "i"(N));
}
template <int NL, int ND> __device__ __forceinline__ void step4(v4u* d, v4u* u, rsrc_t rs, const v4u& vo, u32 soff, u32 t, u32 live) {
  asm volatile(
      "s_cmp_eq_u32 %15, 0\n\ts_cbranch_scc1 dead%=\n" HOLD("%14") LOAD4("%8", "%9", "%10", "%11", "%12", "%13")
      "s_waitcnt vmcnt(%16)\n\ts_branch done%=\n"
      "dead%=:\n\ts_waitcnt vmcnt(%17)\n"
      "done%=:"
      : "+v"(d[0]), "+v"(d[1]), "+v"(d[2]), "+v"(d[3]), "+v"(u[0]), "+v"(u[1]), "+v"(u[2]), "+v"(u[3])
      : "v"(vo.x), "v"(vo.y), "v"(vo.z), "v"(vo.w), "s"(rs), "s"(soff), "s"(t), "s"(live), "i"(NL), "i"(ND) : "s40", "s41", "scc");
}

__global__ void __launch_bounds__(512, 2) fused_block(Params p) {
  __shared__ __attribute__((aligned(16))) Lds L_;
  LAS Lds* L = (LAS Lds*)&L_;
  const int lane = threadIdx.x & 63, wave = __builtin_amdgcn_readfirstlane(threadIdx.x >> 6);
  const int r = lane >> 4, c = lane & 15;
  const int xl = blockIdx.x & 7, l = blockIdx.x >> 3;
  const int bh = (l >> 2) * 8 + xl, b = bh >> 5, h = bh & 31, s = l & 3;
  u32 t0 = now();
  STAMP(0);

  if (wave >= 4) {
    const int w = wave - 4;
    const rsrc_t rk = mkrs(p.K + (size_t)bh * (8192 * 128), 8192 * 256), rv = mkrs(p.V + (size_t)bh * (8192 * 128), 8192 * 256);
    const u32 v0 = s * 16384 + w * 1024 + lane * 16;
    const v4u vo = {v0, v0 + 3072, v0 + 2 * 3072, v0 + 3 * 3072};
    v4u a[48];
    const int pz = due_of(PAUSE_G) + (l < 12 ? PAUSE_F : l >= 24 ? PAUSE_R : PP_);
    auto due = [&](int g) { const int d0 = KSTART + ((g * PACE) >> 4); return g < PAUSE_G ? d0 : max(d0, pz + (((g - PAUSE_G) * FAST) >> 4)); };
    auto issue = [&](auto G) {
      constexpr int g = G;
      hold(t0 + due(g));
      if constexpr (g < 20) asm volatile("s_waitcnt vmcnt(%0)" :: "i"(g < DEPTH_G ? DEPTH - 4 : 28));
      if constexpr (g < 8) dma4(rk, vo, g * 65536, (u32)(uintptr_t)&L->kst[w][4 * g][0]);
      else if constexpr (g < 20) load4_first(a + 4 * slot_of(g), rk, vo, g * 65536);
      else load4(a + 4 * slot_of(g), g < 32 ? rk : rv, vo, (g & 31) * 65536);
      __builtin_amdgcn_sched_barrier(0);
    };
    __builtin_amdgcn_s_barrier();
    Unroll<0, 20>::run(issue);
    STAMP(1);
    lds_wait(&L->cnt[C_Q], 1);
    t0 += __builtin_amdgcn_readfirstlane(L->cnt[C_SHIFT]);
    STAMP(2);
    const v4u q = *(LAS v4u*)&L->qd[4 * c];
    float S[8], x[16];
    auto use = [&](auto G, auto LAST) {
      constexpr int g = G;
      if constexpr (g >= 8) landed<4 * (LAST - g)>(a + 4 * slot_of(g));
#pragma unroll
      for (int k = 0; k < 4; ++k) {
        if constexpr (g < 8) x[4 * (g & 3) + k] = qk(L->kst[w][4 * g + k][lane], q);
        else x[4 * (g & 3) + k] = qk(a[4 * slot_of(g) + k], q);
      }
      if constexpr ((g & 3) == 3) S[g >> 2] = tile_scores(x, lane);
      __builtin_amdgcn_sched_barrier(0);
    };
    Unroll<8, 12>::run([&](auto U) { use(U, std::integral_constant<int, 19>{}); });
    asm volatile("" ::: "memory");
    issue(std::integral_constant<int, 20>{});
    Unroll<0, 4>::run([&](auto U) { use(U, std::integral_constant<int, 20>{}); });
    issue(std::integral_constant<int, 21>{});
    Unroll<4, 8>::run([&](auto U) { use(U, std::integral_constant<int, 21>{}); });
    Unroll<22, 32 + LAG>::run([&](auto G) {
      issue(G);
      Unroll<used_after(G - 1) + 1, used_after(G) + 1>::run([&](auto U) { use(U, G); });
    });
    STAMP(3);
    {
      float m = wave_max(fmaxf(fmaxf(fmaxf(S[0], S[1]), fmaxf(S[2], S[3])), fmaxf(fmaxf(S[4], S[5]), fmaxf(S[6], S[7]))));
      if (lane == 0) L->mw[w] = m;
      lds_arrive(&L->cnt[C_MAX]);
      lds_wait(&L->cnt[C_MAX], 4);
      const v4f m4 = *(LAS v4f*)L->mw;
      const float ml = fmaxf(fmaxf(m4.x, m4.y), fmaxf(m4.z, m4.w)) * 1.4426950408889634f;
      float ls = 0.f;
#pragma unroll
      for (int t = 0; t < 8; ++t) {
        const float pv = __builtin_amdgcn_exp2f(fmaf(S[t], 1.4426950408889634f, -ml));
        ls += pv;
        S[t] = __uint_as_float(pack2(pv, pv - rbf(pv)));
      }
      ls = wave_sum(ls);
      if (lane == 0) L->lw[w] = ls;
      lds_arrive(&L->cnt[C_SUM]);
    }
    v4f acc = {0.f, 0.f, 0.f, 0.f};
    v4u sel;
    {
      const u32 pick = (c & 8) ? 0x0302u : 0x0100u, own = (c & 1) ? (pick << 16) | 0x0c0cu : 0x0c0c0000u | pick;
#pragma unroll
      for (int d = 0; d < 4; ++d) sel[d] = ((c & 7) >> 1) == d ? own : 0x0c0c0c0cu;
    }
#pragma unroll 1
    for (int it = 0; it < 4; ++it) {
      Unroll<0, 8>::run([&](auto J) {
        constexpr int j = J, g = 32 + LAG + j;
        v4u bm[4];
        Unroll<0, 4>::run([&](auto K) { bm[K] = pv_diag<4 * (j & 3) + K>(__float_as_uint(S[j >> 2]), sel); pin(bm[K]); });
        step4<4 * LAG, 4 * (7 - j)>(a + 4 * slot_of(g), a + 4 * slot_of(32 + j), rv, vo, (LAG + j + 8 * it) * 65536, t0 + due(g + 8 * it), j + LAG < 8 ? 1 : 3 - it);
#pragma unroll
        for (int k = 0; k < 4; ++k)
          acc = __builtin_amdgcn_mfma_f32_16x16x32_bf16(__builtin_bit_cast(bf16x8, a[4 * slot_of(32 + j) + k]), __builtin_bit_cast(bf16x8, bm[k]), acc, 0, 0, 0);
        pin(acc);
        __builtin_amdgcn_sched_barrier(0);
      });
#pragma unroll
      for (int t = 0; t < 6; ++t) S[t] = S[t + 2];
    }
    STAMP(4);
    LAS float* o = &L->op[w][32 * r + 2 * (c & 8) + (c & 7)];
    const float o0 = acc.x + dppf<ROR8>(acc.x), o1 = acc.y + dppf<ROR8>(acc.y), o2 = acc.z + dppf<ROR8>(acc.z), o3 = acc.w + dppf<ROR8>(acc.w);
    o[0] = (c & 8) ? o2 : o0; o[8] = (c & 8) ? o3 : o1;
    wg_barrier();
    STAMPS_OUT();
    return;
  }

  const int w = wave, t = w * 64 + lane;
  const rsrc_t rs = mkrs(p.ws, WS_END), rm = mkrs(p.wmod, 512 * 3072 * 2);
  const bool first = l >= 16 && w >= 2;
  v2u c0 = {0u, 0u}, c1 = c0;
  v4u W[32];
  if (first) {
    const int row = 64 * xl + 32 * (w - 2) + 4 * (lane >> 3);
    c0 = ((const GAS v2u*)p.c)[row >> 2]; c1 = ((const GAS v2u*)p.c)[128 + (row >> 2)];
#pragma unroll
    for (int k = 0; k < 4; ++k) W[k] = ldw(rm, row * 6144 + (lane & 7) * 16, k * 6144 + (l - 16) * 128);
  }
  const u32 epoch = __hip_atomic_load((GAS u32*)(p.ws + WS_EPOCH), __ATOMIC_RELAXED, __HIP_MEMORY_SCOPE_AGENT);
  const int j8 = 2 * s + (w & 1);
  v4u xs = {0u, 0u, 0u, 0u};
  u32 xo = 0;
  if (w < 2) {
    xs = ((const GAS v4u*)p.x)[b * 64 + lane];
    xo = ((const GAS u32*)p.x)[b * 256 + 32 * j8 + 8 * r + (c & 7)];
    const rsrc_t rq = mkrs(p.wq, 512 * 4096 * 2);
#pragma unroll
    for (int k = 0; k < 16; ++k) W[k] = ldw(rq, (64 * j8 + 16 * r) * 8192 + h * 256 + c * 16, k * 8192);
  }
  __builtin_amdgcn_sched_barrier(0);
  if (threadIdx.x < 16) L->cnt[threadIdx.x] = 0;
  wg_barrier();
  __builtin_amdgcn_sched_barrier(0);
  if (w == 3 && l == 12) {
    u64 pc;
    asm volatile("s_getpc_b64 %0" : "=s"(pc));
#pragma unroll
    for (int k = 0; k < CODE_KB; ++k) W[k] = ((const GAS v4u*)(pc & ~127ull))[k * 64 + lane];
#pragma unroll
    for (int k = 0; k < CODE_KB; ++k) pin(W[k]);
  }
  const u32 tag = ((u32)__builtin_amdgcn_readfirstlane(epoch) | 1u) + 2u;
  if (l == 0 && w == 3) publish(rs, lane == 0 ? WS_T + xl * 16 : OOB, t0, t0, tag);
  if (first) {
    float v[16] = {0.f, 0.f, 0.f, 0.f, 0.f, 0.f, 0.f, 0.f, 0.f, 0.f, 0.f, 0.f, 0.f, 0.f, 0.f, 0.f};
    rows2(W[0], W[1], silu2(c0.x), silu2(c1.x), v);
    rows2(W[2], W[3], silu2(c0.y), silu2(c1.y), v);
#pragma unroll
    for (int j = 0; j < 8; ++j) v[j] = sum32(v[j], v[j + 8]);
#pragma unroll
    for (int j = 0; j < 4; ++j) v[j] = sum16(v[j], v[j + 4]);
    L->red1[w][lane] = v2f{trstep<ROR8>(v[0], v[2], lane & 8), trstep<ROR8>(v[1], v[3], lane & 8)};
    if (last_of(&L->cnt[C_MP], 2, lane)) {
      const int src = (lane & 32) + (lane & 3) * 8 + ((lane >> 2) & 7);
      publish(rs, WS_MP + ((l - 16) * 8 + xl) * 1024 + lane * 16, L->red1[2][src] + L->red1[3][src], tag);
    }
  }
  STAMP(1);
  if (w < 2) {
    v2f mean, rstd;
    {
      v2f e[8];
#pragma unroll
      for (int k = 0; k < 4; ++k) { e[2 * k] = v2f{bflo(xs[k]), bflo(xs[k])}; e[2 * k + 1] = v2f{bfhi(xs[k]), bfhi(xs[k])}; }
      ln_stats(e, lane, mean, rstd);
      pin(mean); pin(rstd);
    }
#pragma unroll
    for (int k = 0; k < 8; ++k) { interleave(W[2 * k], W[2 * k + 1]); pin(W[2 * k]); pin(W[2 * k + 1]); }
    u32 off[8];
#pragma unroll
    for (int k = 0; k < 8; ++k) off[k] = WS_MP + (((c & 8) + j8) * 8 + k) * 1024 + b * 512 + (8 * r + (c & 7)) * 16;
    v4u val[8];
    hold(t0 + MOD_T);
    gather<8, 2>(rs, off, val, tag, t0);
    STAMP(3);
    v2f m = payload(val[0]);
#pragma unroll
    for (int k = 1; k < 8; ++k) m += payload(val[k]);
    const u32 own = pack2(m.x, m.y), oth = dppu<ROR8>(own);
    const v2f hv = modulate(unpack2(xo), mean, rstd, rbf2(unpack2((c & 8) ? own : oth) + 1.f), unpack2((c & 8) ? oth : own));
    float u[4];
    gemv<8>(W, pack2(hv.x, hv.y), u);
    L->red1[w][lane] = v2f{sum16(u[0], u[2]), sum16(u[1], u[3])};
    if (last_of(&L->cnt[C_QP], 2, lane)) {
      publish(rs, WS_QP + (bh * 4 + s) * 1024 + lane * 16, L->red1[0][lane] + L->red1[1][lane], tag);
      lds_arrive(&L->cnt[C_QPUB]);
    }
  } else if (w == 2) {
    const u32 o0 = WS_QP + bh * 4096 + lane * 16, off[5] = {o0, o0 + 1024, o0 + 2048, o0 + 3072, WS_T + (lane & 7) * 16};
    v4u val[5];
    lds_wait(&L->cnt[C_QPUB], 1);
    __builtin_amdgcn_s_sleep(D_Q);
    gather<5, 2>(rs, off, val, tag, t0);
    STAMP(3);
    int d = (int)(val[4].x - t0);
    d = min(d, (int)dppu<XOR1>(d)); d = min(d, (int)dppu<XOR2>(d)); d = min(d, (int)dppu<HMIR>(d));
    L->cnt[C_SHIFT] = max(min(d, 0), -200);
    const v2f qv = (payload(val[0]) + payload(val[1])) + (payload(val[2]) + payload(val[3]));
    L->qd[4 * c + r] = pack2(qv.x, qv.y);
    lds_arrive(&L->cnt[C_Q]);
  }
  STAMP(4);

  const int fstart = (43 * xl + 3) >> 2, fg = fstart + l;
  const bool ffn = fg < (43 * (xl + 1) + 3) >> 2;
  v4u F[8];
  v4u W2[4];
#pragma unroll
  for (int k = 0; k < 8; ++k) F[k] = v4u{0u, 0u, 0u, 0u};
#pragma unroll
  for (int k = 0; k < 4; ++k) W2[k] = v4u{0u, 0u, 0u, 0u};
  v4u Wo[32];
  {
    const rsrc_t ro = mkrs(p.wo, 4096 * 512 * 2);
    hold(t0 + STRIP_T);
#pragma unroll
    for (int k = 0; k < 8; ++k) Wo[k] = ldw(ro, (h * 128 + 32 * s + 2 * r) * 1024 + w * 256 + c * 16, (k / 2) * 8192 + (k & 1) * 1024);
    __builtin_amdgcn_sched_barrier(0);
  }
  if (l >= 24) {
    const int rg = xl * 8 + l - 24, row = 128 * w + 8 * (lane >> 2);
    const v4u ca = ((const GAS v4u*)p.c)[row >> 3], cb = ((const GAS v4u*)p.c)[64 + (row >> 3)];
#pragma unroll
    for (int k = 0; k < 8; ++k) W[k] = ldw(rm, row * 6144 + (lane & 3) * 16, k * 6144 + 2048 + rg * 64);
    float v[16] = {0.f, 0.f, 0.f, 0.f, 0.f, 0.f, 0.f, 0.f, 0.f, 0.f, 0.f, 0.f, 0.f, 0.f, 0.f, 0.f};
#pragma unroll
    for (int k = 0; k < 4; ++k) rows2(W[2 * k], W[2 * k + 1], silu2(ca[k]), silu2(cb[k]), v);
#pragma unroll
    for (int j = 0; j < 8; ++j) v[j] = sum32(v[j], v[j + 8]);
#pragma unroll
    for (int j = 0; j < 4; ++j) { v[j] = sum16(v[j], v[j + 4]); v[j] += dppf<ROR8>(v[j]); v[j] += dppf<ROR4>(v[j]); }
    if ((lane & 12) == 0) L->red1b[w][4 * r + (lane & 3)] = v4f{v[0], v[1], v[2], v[3]};
    if (last_of(&L->cnt[C_MOD], 4, lane)) {
      const int src = (c & 8) + (c & 1) * 4 + ((c >> 1) & 3);
      const v4f a = (L->red1b[0][src] + L->red1b[1][src]) + (L->red1b[2][src] + L->red1b[3][src]);
      publish(rs, lane < 16 ? WS_MOD + (c >> 3) * 8192 + (8 * rg + (c & 7)) * 16 : OOB, pack2(a.x, a.y), pack2(a.z, a.w), tag);
    }
  } else if (ffn) {
    const rsrc_t r1 = mkrs(p.w1, 512 * 1376 * 2), r3 = mkrs(p.w3, 512 * 1376 * 2), r2 = mkrs(p.w2, 1376 * 512 * 2);
#pragma unroll
    for (int k = 0; k < 8; ++k) F[k] = ldw(k < 4 ? r1 : r3, (128 * w + (lane >> 1)) * 2752 + (lane & 1) * 16, (k & 3) * 32 * 2752 + fg * 32);
#pragma unroll
    for (int k = 0; k < 4; ++k) W2[k] = ldw(r2, r * 4096 + w * 256 + c * 16, (16 * fg + k) * 1024);
    LAS v4s* tr = (LAS v4s*)&L->stage[w][0] + (8 * r + (c >> 2)) * 4 + (c & 3);
#pragma unroll
    for (int k = 0; k < 8; ++k) {
      L->stage[w][lane] = F[k];
      asm volatile("" ::: "memory");
      const v2u lo = __builtin_bit_cast(v2u, __builtin_amdgcn_ds_read_tr16_b64_v4i16(tr));
      const v2u hi = __builtin_bit_cast(v2u, __builtin_amdgcn_ds_read_tr16_b64_v4i16(tr + 16));
      F[k] = v4u{lo.x, lo.y, hi.x, hi.y};
      asm volatile("" : "+v"(F[k]) :: "memory");
    }
    interleave(W2[0], W2[1]); interleave(W2[2], W2[3]);
  }
#pragma unroll
  for (int k = 0; k < 4; ++k) { interleave(Wo[2 * k], Wo[2 * k + 1]); pin(Wo[2 * k]); pin(Wo[2 * k + 1]); }
  lds_wait(&L->cnt[C_Q], 1);
  t0 += __builtin_amdgcn_readfirstlane(L->cnt[C_SHIFT]);

  const bool red = l < 4;
  const int j = xl * 4 + (l & 3), rp = 64 * (j >> 3) + 16 * (j & 3) + 4 * (lane & 3) + 2 * ((j >> 2) & 1) + ((lane >> 2) & 1);
  const int tp = w * 64 + 16 * ((lane >> 3) & 3) + 4 * (lane & 3) + 2 * (lane >> 5) + ((lane >> 2) & 1);
  v2f sc0 = {0.f, 0.f}, sc1 = sc0, sh0 = sc0, sh1 = sc0;
  float xr = 0.f, gar = 0.f, gbr = 0.f, mloc = 0.f;
  {
    {
      hold(t0 + 1600);
      {
        if (ffn) {
          const u32 modb = WS_MOD + (1 + (lane >> 5)) * 2048 + (32 * w + (lane & 31)) * 16;
          const u32 base = WS_MOD + (r >> 1) * 8192 + (rp >> 1) * 16;
          const u32 off[4] = {modb, modb + 8192, (red && w == 0) ? base : OOB, (red && w == 0) ? base + 3 * 2048 : OOB};
          v4u val[4];
          const u32 xv = ((const GAS u32*)p.x)[(r >> 1) * 256 + rp];
          gather<4, 16, 2>(rs, off, val, tag, t0);
          const int src = (tp & 62) * 2;
          auto fetch = [&](int from, const v4u& u) {
            u32 lo = __builtin_amdgcn_ds_bpermute(from, u.x), hi = __builtin_amdgcn_ds_bpermute(from, u.z);
            pin(lo); pin(hi);
            return (tp & 1) ? hi : lo;
          };
          const u32 h0 = fetch(src, val[0]), h1 = fetch(src, val[1]), g0 = fetch(src + 128, val[0]), g1 = fetch(src + 128, val[1]);
          sc0 = rbf2(v2f{bflo(g0), bflo(g1)} + 1.f); sc1 = rbf2(v2f{bfhi(g0), bfhi(g1)} + 1.f);
          sh0 = v2f{bflo(h0), bflo(h1)}; sh1 = v2f{bfhi(h0), bfhi(h1)};
          const u32 ga = (rp & 1) ? val[2].z : val[2].x, gb = (rp & 1) ? val[3].z : val[3].x;
          xr = (r & 1) ? bfhi(xv) : bflo(xv); gar = (r & 1) ? bfhi(ga) : bflo(ga); gbr = (r & 1) ? bfhi(gb) : bflo(gb);
        }
      }
      {
        lds_wait(&L->cnt[C_SUM], 4);
        const v4f m4 = *(LAS v4f*)L->mw, l4 = *(LAS v4f*)L->lw;
        mloc = fmaxf(fmaxf(m4.x, m4.y), fmaxf(m4.z, m4.w));
        publish(rs, t == 0 ? WS_ML + (bh * 4 + s) * 16 : OOB, v2f{mloc, (l4.x + l4.y) + (l4.z + l4.w)}, tag);
      }
    }
  }
  __builtin_amdgcn_s_sleep(60);
  STAMP(5);
  float scale;
  {
    const u32 off[1] = {WS_ML + (bh * 4 + (lane & 3)) * 16};
    v4u val[1];
    gather<1, 16>(rs, off, val, tag, t0);
    const v2f ml = payload(val[0]);
    float M = fmaxf(ml.x, dppf<XOR1>(ml.x)); M = fmaxf(M, dppf<XOR2>(M));
    float Ls = ml.y * __builtin_amdgcn_exp2f((ml.x - M) * 1.4426950408889634f);
    Ls += dppf<XOR1>(Ls); Ls += dppf<XOR2>(Ls);
    scale = __builtin_amdgcn_exp2f((mloc - M) * 1.4426950408889634f) / Ls;
  }
  STAMP(6);

  u32 o_y = (r & 1) ? OOB : WS_Y + ((8 * w + 2 * r + (c >> 2)) * 256 + bh * 4 + s) * 64 + (c & 3) * 16;
  u32 o_yg = WS_Y + j * 16384 + w * 4096 + lane * 16;
  u32 o_x = (lane >> 3) < REP_X1 ? WS_X1 + (lane >> 3) * 4096 + j * 128 + (lane & 7) * 16 : OOB;
  u32 o_xg[4];
#pragma unroll
  for (int k = 0; k < 4; ++k) { o_xg[k] = WS_X1 + (xl & (REP_X1 - 1)) * 4096 + lane * 16 + ((w + k) & 3) * 1024; pin(o_xg[k]); }
  u32 o_o = WS_OP + ((8 * w + 4 * (r >> 1) + (c >> 2)) * 96 + fg) * 128 + (r & 1) * 64 + (c & 3) * 16;
  u32 o_og[3];
  o_og[0] = WS_OP + j * 12288 + w * 3072 + lane * 16;
  o_og[1] = (w < 3 || (lane >> 3) < NFFN - 80) ? o_og[0] + 1024 : OOB;
  o_og[2] = w < 3 ? o_og[0] + 2048 : OOB;
  u32 o_e = (j == 0 && lane == 0) ? WS_EPOCH : OOB;
  u32 o_out = (lane & 8) == 0 ? ((r >> 1) * 512 + 2 * rp + (r & 1)) * 2 : OOB;
  const rsrc_t rout = mkrs(p.out, 2048);
  LAS v2f* a_o = (LAS v2f*)&L->op[0][32 * r + 8 * (c & 3) + 2 * (c >> 2)];
  u32 o_h = WS_O + (bh * 4 + s) * 1024 + lane * 16, o_hg[1] = {WS_O + (bh * 4 + (c >> 2)) * 1024 + (16 * s + 4 * r + (c & 3)) * 16};
  pin(o_h); pin(o_hg[0]);
  LAS float* a_rw = &L->ry[w][4 * r + (lane & 3)];
  LAS float* a_rr = &L->ry[r & 2][2 * (lane & 4) + 4 * (r & 1) + (lane & 3)];
  LAS u32* a_hw = &L->h2[0][tp];
  LAS v4u* a_hr = (LAS v4u*)&L->h2[c & 1][64 * w + 4 * r];
  LAS float* a_gw = &L->red2[lane][w];
  LAS v4f* a_gr = (LAS v4f*)L->red2[(c & 4) * 4 + 4 * r + (c & 3)];
  LAS float* a_ow = &L->ro[w][8 * r + (lane & 7)];
  LAS float* a_or = &L->ro[0][8 * r + (lane & 7)];
  pin(o_y); pin(o_yg); pin(o_x); pin(o_o); pin(o_og[0]); pin(o_og[1]); pin(o_og[2]); pin(o_e); pin(o_out);
  pin(scale); pin(sc0); pin(sc1); pin(sh0); pin(sh1); pin(xr); pin(gar); pin(gbr);
  pin(a_o); pin(a_rw); pin(a_rr); pin(a_hw); pin(a_hr); pin(a_gw); pin(a_gr); pin(a_ow); pin(a_or);

  wg_barrier();
  STAMP(7);
  {
    if (w == 0) publish(rs, o_h, ((a_o[0] + a_o[64]) + (a_o[128] + a_o[192])) * scale, tag);
    __builtin_amdgcn_s_sleep(D_H);
    v4u val[1];
    gather<1, 0>(rs, o_hg, val, tag, t0);
    float o0 = __uint_as_float(val[0].x), o1 = __uint_as_float(val[0].z);
    o0 += dppf<ROR4>(o0); o1 += dppf<ROR4>(o1); o0 += dppf<ROR8>(o0); o1 += dppf<ROR8>(o1);
    float u[4];
    gemv<4>(Wo, pack2(o0, o1), u);
    publish(rs, o_y, pack2(sum16(u[0], u[0]), sum16(u[1], u[1])), pack2(sum16(u[2], u[2]), sum16(u[3], u[3])), tag);
  }
  STAMP(8);
  if (ffn) {
    float x1r = 0.f;
    {
      const u32 off[4] = {o_yg, o_yg + 1024, o_yg + 2048, o_yg + 3072};
      v4u val[4];
      __builtin_amdgcn_s_sleep(D_Y);
      gather<4, 0>(rs, off, val, tag, t0);
      STAMP(9);
      const v2f lo = (unpack2(val[0].x) + unpack2(val[1].x)) + (unpack2(val[2].x) + unpack2(val[3].x));
      const v2f hi = (unpack2(val[0].z) + unpack2(val[1].z)) + (unpack2(val[2].z) + unpack2(val[3].z));
      float a = sum16(sum32(lo.x, hi.x), sum32(lo.y, hi.y));
      a += dppf<ROR8>(a); a += dppf<ROR4>(a);
      a_rw[0] = a;
      wg_barrier();
      if (red && w == 0) {
        x1r = rbf(xr + rbf(gar * rbf(a_rr[0] + a_rr[16])));
        const auto e = __builtin_amdgcn_permlane16_swap(__float_as_uint(x1r), __float_as_uint(x1r), false, false);
        const u32 pr = (e[0] >> 16) | e[1];
        const auto bb = __builtin_amdgcn_permlane32_swap(pr, pr, false, false);
        publish(rs, o_x, bb[0], bb[1], tag);
        __builtin_amdgcn_raw_buffer_store_b32(tag, rs, o_e, 0, AUX_SC1);
        STAMP(10);
        __builtin_amdgcn_s_sleep(D_XR);
      } else __builtin_amdgcn_s_sleep(D_X);
    }
    v4u val[4];
    gather<4, 0>(rs, o_xg, val, tag, t0);
    STAMP(11);
    v2f e[8], mean, rstd;
#pragma unroll
    for (int k = 0; k < 4; ++k) { e[2 * k] = v2f{bflo(val[k].x), bflo(val[k].z)}; e[2 * k + 1] = v2f{bfhi(val[k].x), bfhi(val[k].z)}; }
    ln_stats(e, lane, mean, rstd);
    const v2f ha = modulate(e[0], mean, rstd, sc0, sh0), hb = modulate(e[1], mean, rstd, sc1, sh1);
    a_hw[0] = pack2(ha.x, hb.x); a_hw[256] = pack2(ha.y, hb.y);
    asm volatile("" ::: "memory");
    v4u A[4];
#pragma unroll
    for (int k = 0; k < 4; ++k) A[k] = a_hr[4 * k];
    __builtin_amdgcn_sched_barrier(0);
    v4f dg = {0.f, 0.f, 0.f, 0.f}, du = dg;
#pragma unroll
    for (int k = 0; k < 4; ++k) {
      dg = __builtin_amdgcn_mfma_f32_16x16x32_bf16(__builtin_bit_cast(bf16x8, A[k]), __builtin_bit_cast(bf16x8, F[k]), dg, 0, 0, 0);
      du = __builtin_amdgcn_mfma_f32_16x16x32_bf16(__builtin_bit_cast(bf16x8, A[k]), __builtin_bit_cast(bf16x8, F[4 + k]), du, 0, 0, 0);
    }
    const float lo = (lane & 16) ? dg.y : dg.x, hi = (lane & 16) ? du.y : du.x;
    a_gw[0] = (lane & 32) ? hi : lo;
    wg_barrier();
    const v4f g4 = a_gr[0], u4 = a_gr[32];
    const v2f gu = rbf2(v2f{(g4.x + g4.y) + (g4.z + g4.w), (u4.x + u4.y) + (u4.z + u4.w)});
    const float hh = rbf(silu(gu.x)) * gu.y;
    u32 hp = pack2(hh, dppf<XOR1>(hh));
    float o[16] = {0.f, 0.f, 0.f, 0.f, 0.f, 0.f, 0.f, 0.f, 0.f, 0.f, 0.f, 0.f, 0.f, 0.f, 0.f, 0.f};
    dpp_ready(hp);
    dot8<0>(W2[0], W2[1], hp, o); dot8<4>(W2[0], W2[1], hp, o + 8); dot8<2>(W2[2], W2[3], hp, o); dot8<6>(W2[2], W2[3], hp, o + 8);
    dots_done(o); dots_done(o + 8);
#pragma unroll
    for (int k = 0; k < 4; ++k) { o[k] = sum32(o[k], o[k + 4]); o[8 + k] = sum32(o[8 + k], o[12 + k]); }
    publish(rs, o_o, pack2(sum16(o[0], o[2]), sum16(o[1], o[3])), pack2(sum16(o[8], o[10]), sum16(o[9], o[11])), tag);
    STAMP(12);
    if (red) {
      v4u va[3];
      __builtin_amdgcn_s_sleep(D_O);
      gather<3, 0, 1>(rs, o_og, va, tag, t0);
      STAMP(13);
      const v2f b0 = unpack2(va[0].x) + unpack2(va[1].x) + unpack2(va[2].x), b1 = unpack2(va[0].z) + unpack2(va[1].z) + unpack2(va[2].z);
      float a = sum16(sum32(b0.x, b1.x), sum32(b0.y, b1.y));
      a += dppf<ROR8>(a);
      a_ow[0] = a;
      wg_barrier();
      if (w == 0) {
        const float res = x1r + rbf(gbr * rbf((a_or[0] + a_or[32]) + (a_or[64] + a_or[96])));
        __builtin_amdgcn_raw_buffer_store_b16((u16)pack2(res, res), rout, o_out, 0, 0);
        STAMP(14);
      }
    }
  }
  STAMPS_OUT();
}

extern "C" void kernel_launch(void* const* d_in, const int* in_sizes, int n_in, void* d_out, int out_size, void* d_ws, size_t ws_size, hipStream_t stream) {
  Params p;
  p.x = (const u16*)d_in[0]; p.K = (const u16*)d_in[1]; p.V = (const u16*)d_in[2]; p.wq = (const u16*)d_in[3]; p.wo = (const u16*)d_in[4];
  p.w1 = (const u16*)d_in[6]; p.w3 = (const u16*)d_in[7]; p.w2 = (const u16*)d_in[8]; p.c = (const u16*)d_in[9]; p.wmod = (const u16*)d_in[10];
  p.out = (u16*)d_out; p.ws = (unsigned char*)d_ws;
  hipLaunchKernelGGL(fused_block, dim3(256), dim3(512), 0, stream, p);
}
```

```cpp
#include <hip/hip_runtime.h>
#include <stdint.h>
#include <type_traits>

typedef unsigned short u16;
typedef unsigned int u32;
typedef unsigned long long u64;
typedef u32 v4u __attribute__((ext_vector_type(4)));
typedef u32 v2u __attribute__((ext_vector_type(2)));
typedef float v4f __attribute__((ext_vector_type(4)));
typedef float v2f __attribute__((ext_vector_type(2)));
typedef __bf16 bf16x2 __attribute__((ext_vector_type(2)));
typedef __bf16 bf16x8 __attribute__((ext_vector_type(8)));
typedef short v4s __attribute__((ext_vector_type(4)));
typedef __amdgpu_buffer_rsrc_t rsrc_t;
#define GAS __attribute__((address_space(1)))
#define LAS __attribute__((address_space(3)))

constexpr int KSTART = 100;
constexpr int PACE = 960;
constexpr int DEPTH = 8, DEPTH_G = 10;
constexpr int LAG = 7;
constexpr int PAUSE_G = 12, PAUSE_F = 370, PAUSE_R = 200, FAST = 810;
constexpr int MOD_T = 320, LATE_T = 830, MOD2_T = 1600;
constexpr int D_Q = 30, D_ML = 60, D_H = 15, D_Y = 16, D_X = 19, D_XR = 16, D_O = 16;
constexpr u32 SPIN_TICKS = 400000;
constexpr int CODE_KB = 27;
constexpr int slot_of(int g) { return g < 20 ? g - 8 : (g - 20) & 7; }
constexpr int used_after(int g) { return (g <= 21 || g - LAG < 11) ? 11 : g - LAG; }

constexpr int NFFN = 86, REP_X1 = 4;
constexpr u32 WS_EPOCH = 0;
constexpr u32 WS_MP = 256;
constexpr u32 WS_MOD = WS_MP + 16 * 8 * 1024;
constexpr u32 WS_ML = WS_MOD + 2 * 8192;
constexpr u32 WS_T = WS_ML + 4096;
constexpr u32 WS_X1 = WS_T + 128;
constexpr u32 WS_QP = WS_X1 + REP_X1 * 4096;
constexpr u32 WS_O = WS_QP + 256 * 1024;
constexpr u32 WS_Y = WS_O + 256 * 1024;
constexpr u32 WS_OP = WS_Y + 32 * 256 * 64;
constexpr u32 WS_END = WS_OP + 32 * 96 * 128;
constexpr u32 OOB = 0x7ffffff0u;
constexpr int AUX_SC1 = 16, AUX_NT = 2;

struct Params {
  const u16 *x, *K, *V, *wq, *wo, *w1, *w3, *w2, *c, *wmod;
  u16* out;
  unsigned char* ws;
};

enum { C_MP, C_Q, C_MAX, C_SUM, C_MOD, C_QP, C_QPUB, C_SHIFT };
struct Lds {
  u32 cnt[16];
  v2f red1[4][64];
  v4f red1b[4][16];
  u32 qd[64];
  float mw[4], lw[4];
  float op[4][128];
  float red2[64][4];
  u32 h2[2][256];
  float ry[4][16], ro[4][32];
  v4u stage[4][64];
  v4u kst[4][32][64];
};

__device__ __forceinline__ float bflo(u32 v) { return __uint_as_float(v << 16); }
__device__ __forceinline__ float bfhi(u32 v) { return __uint_as_float(v & 0xffff0000u); }
__device__ __forceinline__ v2f unpack2(u32 v) { return v2f{bflo(v), bfhi(v)}; }
__device__ __forceinline__ u32 pack2(float a, float b) { bf16x2 r = {(__bf16)a, (__bf16)b}; return __builtin_bit_cast(u32, r); }
__device__ __forceinline__ float rbf(float a) { return bflo(pack2(a, a)); }
__device__ __forceinline__ v2f rbf2(v2f v) { u32 r = pack2(v.x, v.y); asm("" : "+v"(r)); return unpack2(r); }
__device__ __forceinline__ float dot2(u32 a, u32 b, float acc) {
  return __builtin_amdgcn_fdot2_f32_bf16(__builtin_bit_cast(bf16x2, a), __builtin_bit_cast(bf16x2, b), acc, false);
}
__device__ __forceinline__ float silu(float v) { return v * __builtin_amdgcn_rcpf(1.f + __expf(-v)); }
__device__ __forceinline__ u32 silu2(u32 v) { return pack2(silu(bflo(v)), silu(bfhi(v))); }
template <int CTRL> __device__ __forceinline__ u32 dppu(u32 x) { return (u32)__builtin_amdgcn_mov_dpp((int)x, CTRL, 0xf, 0xf, true); }
template <int CTRL> __device__ __forceinline__ float dppf(float x) { return __uint_as_float(dppu<CTRL>(__float_as_uint(x))); }
constexpr int XOR1 = 0xB1, XOR2 = 0x4E, HMIR = 0x141, ROR4 = 0x124, ROR8 = 0x128, BCAST = 0x150;
__device__ __forceinline__ float sum32(float a, float b) {
  auto s = __builtin_amdgcn_permlane32_swap(__float_as_uint(a), __float_as_uint(b), false, false);
  return __uint_as_float(s[0]) + __uint_as_float(s[1]);
}
__device__ __forceinline__ float sum16(float a, float b) {
  auto s = __builtin_amdgcn_permlane16_swap(__float_as_uint(a), __float_as_uint(b), false, false);
  return __uint_as_float(s[0]) + __uint_as_float(s[1]);
}
template <int CTRL> __device__ __forceinline__ float trstep(float lo, float hi, bool bit) {
  const float a = lo + dppf<CTRL>(lo), b = hi + dppf<CTRL>(hi);
  return bit ? b : a;
}
__device__ __forceinline__ float wave_sum(float x) {
  x = sum32(x, x); x = sum16(x, x);
  x += dppf<ROR8>(x); x += dppf<HMIR>(x); x += dppf<XOR2>(x); x += dppf<XOR1>(x);
  return x;
}
__device__ __forceinline__ float wave_max(float x) {
  x = fmaxf(x, dppf<XOR1>(x)); x = fmaxf(x, dppf<XOR2>(x)); x = fmaxf(x, dppf<HMIR>(x)); x = fmaxf(x, dppf<ROR8>(x));
  auto s = __builtin_amdgcn_permlane16_swap(__float_as_uint(x), __float_as_uint(x), false, false);
  x = fmaxf(__uint_as_float(s[0]), __uint_as_float(s[1]));
  auto t = __builtin_amdgcn_permlane32_swap(__float_as_uint(x), __float_as_uint(x), false, false);
  return fmaxf(__uint_as_float(t[0]), __uint_as_float(t[1]));
}
__device__ __forceinline__ rsrc_t mkrs(const void* p, u32 bytes) { return __builtin_amdgcn_make_buffer_rsrc((void*)p, 0, bytes, 0x00020000); }
__device__ __forceinline__ v4u ldw(rsrc_t rs, u32 voff, u32 soff) { return __builtin_amdgcn_raw_buffer_load_b128(rs, voff, soff, AUX_NT); }
__device__ __forceinline__ u32 now() { return (u32)__builtin_amdgcn_s_memrealtime(); }
template <class T> __device__ __forceinline__ void pin(T& v) { asm volatile("" : "+v"(v)); }
template <int I, int E> struct Unroll {
  template <class F> static __device__ __forceinline__ void run(F&& f) {
    if constexpr (I < E) { f(std::integral_constant<int, I>{}); Unroll<I + 1, E>::run(f); }
  }
};

__device__ __forceinline__ void lds_arrive(LAS u32* c) {
  asm volatile("s_mov_b64 exec, 1\n\tds_add_u32 %0, %1\n\ts_mov_b64 exec, -1" :: "v"((u32)(uintptr_t)c), "v"(1u) : "memory");
}
__device__ __forceinline__ void lds_wait(LAS u32* c, u32 n) {
  while ((u32)__builtin_amdgcn_readfirstlane(__hip_atomic_load(c, __ATOMIC_RELAXED, __HIP_MEMORY_SCOPE_WORKGROUP)) < n) __builtin_amdgcn_s_sleep(1);
  asm volatile("" ::: "memory");
}
__device__ __forceinline__ bool last_of(LAS u32* c, u32 n, int lane) {
  asm volatile("" ::: "memory");
  u32 old = 0;
  if (lane == 0) old = __hip_atomic_fetch_add(c, 1u, __ATOMIC_RELAXED, __HIP_MEMORY_SCOPE_WORKGROUP);
  const bool last = (u32)__builtin_amdgcn_readfirstlane(old) == n - 1u;
  asm volatile("" ::: "memory");
  return last;
}
__device__ __forceinline__ void wg_barrier() { asm volatile("s_waitcnt lgkmcnt(0)\n\ts_barrier" ::: "memory"); }

__device__ __forceinline__ void publish(rsrc_t rs, u32 off, u32 a, u32 b, u32 tag) {
  __builtin_amdgcn_raw_buffer_store_b128(v4u{a, tag, b, tag}, rs, off, 0, AUX_SC1);
}
__device__ __forceinline__ void publish(rsrc_t rs, u32 off, v2f v, u32 tag) { publish(rs, off, __float_as_uint(v.x), __float_as_uint(v.y), tag); }
template <int N, int SLEEP, int NFULL = N> __device__ __forceinline__ void gather(rsrc_t rs, const u32 (&off)[N], v4u (&val)[N], u32 tag, u32 t0) {
  for (;;) {
#pragma unroll
    for (int k = 0; k < N; ++k) val[k] = __builtin_amdgcn_raw_buffer_load_b128(rs, off[k], 0, AUX_SC1);
    u32 bad = 0u;
#pragma unroll
    for (int k = 0; k < N; ++k) {
      const u32 m = (val[k].y ^ tag) | (val[k].w ^ tag);
      bad |= (k >= NFULL && off[k] == OOB) ? 0u : m;
    }
    asm("" : "+v"(bad));
    if (__builtin_expect(!__any(bad != 0u), 1) || now() - t0 > SPIN_TICKS) break;
    if (SLEEP) __builtin_amdgcn_s_sleep(SLEEP);
  }
}
__device__ __forceinline__ v2f payload(const v4u& u) { return v2f{__uint_as_float(u.x), __uint_as_float(u.z)}; }

__device__ __forceinline__ void interleave(v4u& a, v4u& b) {
  const v4u lo = {__builtin_amdgcn_perm(b.x, a.x, 0x05040100u), __builtin_amdgcn_perm(b.x, a.x, 0x07060302u),
                  __builtin_amdgcn_perm(b.y, a.y, 0x05040100u), __builtin_amdgcn_perm(b.y, a.y, 0x07060302u)};
  const v4u hi = {__builtin_amdgcn_perm(b.z, a.z, 0x05040100u), __builtin_amdgcn_perm(b.z, a.z, 0x07060302u),
                  __builtin_amdgcn_perm(b.w, a.w, 0x05040100u), __builtin_amdgcn_perm(b.w, a.w, 0x07060302u)};
  a = lo; b = hi;
}
__device__ __forceinline__ void rows2(v4u a, v4u b, u32 s0, u32 s1, float (&v)[16]) {
  interleave(a, b);
#pragma unroll
  for (int j = 0; j < 4; ++j) {
    v[j] = dot2(a[j], s0, v[j]); v[4 + j] = dot2(b[j], s0, v[4 + j]);
    v[8 + j] = dot2(a[j], s1, v[8 + j]); v[12 + j] = dot2(b[j], s1, v[12 + j]);
  }
}
template <int LANE> __device__ __forceinline__ void dot8(const v4u& wa, const v4u& wb, u32 vp, float* a) {
#define DOT(i, j) "v_dot2c_f32_bf16_dpp %" #i ", %8, %" #j " row_newbcast:%c17 row_mask:0xf bank_mask:0xf\n\t"
  asm(DOT(0, 9) DOT(1, 10) DOT(2, 11) DOT(3, 12) DOT(4, 13) DOT(5, 14) DOT(6, 15) DOT(7, 16)
      : "+v"(a[0]), "+v"(a[1]), "+v"(a[2]), "+v"(a[3]), "+v"(a[4]), "+v"(a[5]), "+v"(a[6]), "+v"(a[7])
      : "v"(vp), "v"(wa.x), "v"(wa.y), "v"(wa.z), "v"(wa.w), "v"(wb.x), "v"(wb.y), "v"(wb.z), "v"(wb.w), "i"(LANE));
#undef DOT
}
__device__ __forceinline__ void dpp_ready(u32& vp) { asm("s_nop 1" : "+v"(vp)); }
__device__ __forceinline__ void dots_done(float* a) {
  asm("s_nop 2" : "+v"(a[0]), "+v"(a[1]), "+v"(a[2]), "+v"(a[3]), "+v"(a[4]), "+v"(a[5]), "+v"(a[6]), "+v"(a[7]));
}
template <int NK> __device__ __forceinline__ void gemv(const v4u* W, u32 vp, float (&u)[4]) {
  float a[8] = {0.f, 0.f, 0.f, 0.f, 0.f, 0.f, 0.f, 0.f};
  dpp_ready(vp);
  Unroll<0, NK>::run([&](auto K) { dot8<K>(W[2 * K], W[2 * K + 1], vp, a); });
  dots_done(a);
#pragma unroll
  for (int j = 0; j < 4; ++j) u[j] = sum32(a[j], a[j + 4]);
}

__device__ __forceinline__ void ln_stats(const v2f (&e)[8], int lane, v2f& mean, v2f& rstd) {
  const v2f piv = {__uint_as_float(__builtin_amdgcn_readlane(__float_as_uint(e[0].x), 0)), __uint_as_float(__builtin_amdgcn_readlane(__float_as_uint(e[0].y), 0))};
  v2f d[8];
#pragma unroll
  for (int j = 0; j < 8; ++j) d[j] = e[j] - piv;
  const v2f s = ((d[0] + d[1]) + (d[2] + d[3])) + ((d[4] + d[5]) + (d[6] + d[7]));
  v2f qa = d[0] * d[0], qb = d[1] * d[1];
#pragma unroll
  for (int j = 2; j < 8; j += 2) { qa += d[j] * d[j]; qb += d[j + 1] * d[j + 1]; }
  qa += qb;
  float a = sum16(sum32(s.x, s.y), sum32(qa.x, qa.y));
  a += dppf<ROR8>(a); a += dppf<HMIR>(a); a += dppf<XOR2>(a); a += dppf<XOR1>(a);
  const auto t = __builtin_amdgcn_permlane16_swap(__float_as_uint(a), __float_as_uint(a), false, false);
  const float md = __uint_as_float(t[0]) * (1.f / 512.f), var = fmaxf(fmaf(-md, md, __uint_as_float(t[1]) * (1.f / 512.f)), 0.f);
  const v2f mv = rbf2(v2f{md + (lane < 32 ? piv.x : piv.y), var});
  const float rs = __builtin_amdgcn_rcpf(rbf(__builtin_amdgcn_sqrtf(rbf(mv.y + 1e-5f))));
  const auto m2 = __builtin_amdgcn_permlane32_swap(__float_as_uint(mv.x), __float_as_uint(mv.x), false, false);
  const auto r2 = __builtin_amdgcn_permlane32_swap(__float_as_uint(rs), __float_as_uint(rs), false, false);
  mean = v2f{__uint_as_float(m2[0]), __uint_as_float(m2[1])};
  rstd = v2f{__uint_as_float(r2[0]), __uint_as_float(r2[1])};
}
__device__ __forceinline__ v2f modulate(v2f x, v2f mean, v2f rstd, v2f scale1, v2f shift) { return rbf2(rbf2(rbf2(x - mean) * rstd) * scale1) + shift; }

__device__ __forceinline__ float qk(const v4u k, const v4u q) { return dot2(k.w, q.w, dot2(k.z, q.z, dot2(k.y, q.y, dot2(k.x, q.x, 0.f)))); }
__device__ __forceinline__ float tile_scores(float (&x)[16], int lane) {
#pragma unroll
  for (int j = 0; j < 8; ++j) x[j] = trstep<ROR8>(x[j], x[j + 8], lane & 8);
#pragma unroll
  for (int j = 0; j < 4; ++j) x[j] = trstep<HMIR>(x[j], x[j + 4], lane & 4);
#pragma unroll
  for (int j = 0; j < 2; ++j) x[j] = trstep<XOR2>(x[j], x[j + 2], lane & 2);
  const float d = trstep<XOR1>(x[0], x[1], lane & 1);
  return rbf(rbf(d) * 0.08838834764831845f);
}
template <int J> __device__ __forceinline__ v4u pv_diag(u32 phl, const v4u& sel) {
  const u32 t = dppu<BCAST + J>(phl);
  return v4u{__builtin_amdgcn_perm(t, t, sel.x), __builtin_amdgcn_perm(t, t, sel.y), __builtin_amdgcn_perm(t, t, sel.z), __builtin_amdgcn_perm(t, t, sel.w)};
}
#define HOLD(T) "pace%=:\n\ts_memrealtime s[40:41]\n\ts_waitcnt lgkmcnt(0)\n\ts_sub_u32 s40, s40, " T "\n\ts_cmp_ge_i32 s40, 0\n\t" \
                "s_cbranch_scc1 paced%=\n\ts_sleep 1\n\ts_branch pace%=\npaced%=:\n\t"
__device__ __forceinline__ void hold(u32 t) { asm volatile(HOLD("%0") :: "s"(t) : "s40", "s41", "scc"); }
__device__ __forceinline__ void dma4(rsrc_t rs, const v4u& vo, u32 soff, u32 lds) {
  u32 keep;
  asm volatile(
      "s_mov_b32 %0, m0\n\ts_mov_b32 m0, %7\n\ts_nop 0\n\t"
      "buffer_load_dwordx4 %1, %5, %6 offen nt lds\n\tbuffer_load_dwordx4 %2, %5, %6 offen offset:1024 nt lds\n\t"
      "buffer_load_dwordx4 %3, %5, %6 offen offset:2048 nt lds\n\tbuffer_load_dwordx4 %4, %5, %6 offen offset:3072 nt lds\n\t"
      "s_mov_b32 m0, %0"
      : "=&s"(keep) : "v"(vo.x), "v"(vo.y), "v"(vo.z), "v"(vo.w), "s"(rs), "s"(soff), "s"(lds) : "memory");
}
#define LOAD4(A, B, C, D, RS, SO) \
  "buffer_load_dwordx4 %0, " A ", " RS ", " SO " offen nt\n\tbuffer_load_dwordx4 %1, " B ", " RS ", " SO " offen offset:1024 nt\n\t" \
  "buffer_load_dwordx4 %2, " C ", " RS ", " SO " offen offset:2048 nt\n\tbuffer_load_dwordx4 %3, " D ", " RS ", " SO " offen offset:3072 nt\n\t"
__device__ __forceinline__ void load4_first(v4u* d, rsrc_t rs, const v4u& vo, u32 soff) {
  asm volatile(LOAD4("%4", "%5", "%6", "%7", "%8", "%9")
               : "=&v"(d[0]), "=&v"(d[1]), "=&v"(d[2]), "=&v"(d[3]) : "v"(vo.x), "v"(vo.y), "v"(vo.z), "v"(vo.w), "s"(rs), "s"(soff));
}
__device__ __forceinline__ void load4(v4u* d, rsrc_t rs, const v4u& vo, u32 soff) {
  asm volatile(LOAD4("%4", "%5", "%6", "%7", "%8", "%9")
               : "+v"(d[0]), "+v"(d[1]), "+v"(d[2]), "+v"(d[3]) : "v"(vo.x), "v"(vo.y), "v"(vo.z), "v"(vo.w), "s"(rs), "s"(soff));
}
template <int N> __device__ __forceinline__ void landed(v4u* d) {
  asm volatile("s_waitcnt vmcnt(%4)" : "+v"(d[0]), "+v"(d[1]), "+v"(d[2]), "+v"(d[3]) : "i"(N));
}
template <int NL, int ND> __device__ __forceinline__ void step4(v4u* d, v4u* u, rsrc_t rs, const v4u& vo, u32 soff, u32 t, u32 live) {
  asm volatile(
      "s_cmp_eq_u32 %15, 0\n\ts_cbranch_scc1 dead%=\n" HOLD("%14") LOAD4("%8", "%9", "%10", "%11", "%12", "%13")
      "s_waitcnt vmcnt(%16)\n\ts_branch done%=\n"
      "dead%=:\n\ts_waitcnt vmcnt(%17)\n"
      "done%=:"
      : "+v"(d[0]), "+v"(d[1]), "+v"(d[2]), "+v"(d[3]), "+v"(u[0]), "+v"(u[1]), "+v"(u[2]), "+v"(u[3])
      : "v"(vo.x), "v"(vo.y), "v"(vo.z), "v"(vo.w), "s"(rs), "s"(soff), "s"(t), "s"(live), "i"(NL), "i"(ND) : "s40", "s41", "scc");
}

__global__ void __launch_bounds__(512, 2) fused_block(Params p) {
  __shared__ __attribute__((aligned(16))) Lds L_;
  LAS Lds* L = (LAS Lds*)&L_;
  const int lane = threadIdx.x & 63, wave = __builtin_amdgcn_readfirstlane(threadIdx.x >> 6);
  const int r = lane >> 4, c = lane & 15;
  const int xl = blockIdx.x & 7, l = blockIdx.x >> 3;
  const int bh = (l >> 2) * 8 + xl, b = bh >> 5, h = bh & 31, s = l & 3;
  u32 t0 = now();

  if (wave >= 4) {
    const int w = wave - 4;
    const rsrc_t rk = mkrs(p.K + (size_t)bh * (8192 * 128), 8192 * 256), rv = mkrs(p.V + (size_t)bh * (8192 * 128), 8192 * 256);
    const u32 v0 = s * 16384 + w * 1024 + lane * 16;
    const v4u vo = {v0, v0 + 3072, v0 + 2 * 3072, v0 + 3 * 3072};
    const int resume = KSTART + ((PAUSE_G * PACE) >> 4) + (l < 12 ? PAUSE_F : l >= 24 ? PAUSE_R : 0);
    auto due = [&](int g) { const int d = KSTART + ((g * PACE) >> 4); return g < PAUSE_G ? d : max(d, resume + (((g - PAUSE_G) * FAST) >> 4)); };
    v4u a[48];
    auto issue = [&](auto G) {
      constexpr int g = G;
      hold(t0 + due(g));
      if constexpr (g < 20) asm volatile("s_waitcnt vmcnt(%0)" :: "i"(g < DEPTH_G ? DEPTH - 4 : 28));
      if constexpr (g < 8) dma4(rk, vo, g * 65536, (u32)(uintptr_t)&L->kst[w][4 * g][0]);
      else if constexpr (g < 20) load4_first(a + 4 * slot_of(g), rk, vo, g * 65536);
      else load4(a + 4 * slot_of(g), g < 32 ? rk : rv, vo, (g & 31) * 65536);
      __builtin_amdgcn_sched_barrier(0);
    };
    __builtin_amdgcn_s_barrier();
    Unroll<0, 20>::run(issue);
    lds_wait(&L->cnt[C_Q], 1);
    t0 += __builtin_amdgcn_readfirstlane(L->cnt[C_SHIFT]);
    const v4u q = *(LAS v4u*)&L->qd[4 * c];
    float S[8], x[16];
    auto use = [&](auto G, auto LAST) {
      constexpr int g = G;
      if constexpr (g >= 8) landed<4 * (LAST - g)>(a + 4 * slot_of(g));
#pragma unroll
      for (int k = 0; k < 4; ++k) {
        if constexpr (g < 8) x[4 * (g & 3) + k] = qk(L->kst[w][4 * g + k][lane], q);
        else x[4 * (g & 3) + k] = qk(a[4 * slot_of(g) + k], q);
      }
      if constexpr ((g & 3) == 3) S[g >> 2] = tile_scores(x, lane);
      __builtin_amdgcn_sched_barrier(0);
    };
    Unroll<8, 12>::run([&](auto U) { use(U, std::integral_constant<int, 19>{}); });
    asm volatile("" ::: "memory");
    issue(std::integral_constant<int, 20>{});
    Unroll<0, 4>::run([&](auto U) { use(U, std::integral_constant<int, 20>{}); });
    issue(std::integral_constant<int, 21>{});
    Unroll<4, 8>::run([&](auto U) { use(U, std::integral_constant<int, 21>{}); });
    Unroll<22, 32 + LAG>::run([&](auto G) {
      issue(G);
      Unroll<used_after(G - 1) + 1, used_after(G) + 1>::run([&](auto U) { use(U, G); });
    });
    {
      float m = wave_max(fmaxf(fmaxf(fmaxf(S[0], S[1]), fmaxf(S[2], S[3])), fmaxf(fmaxf(S[4], S[5]), fmaxf(S[6], S[7]))));
      if (lane == 0) L->mw[w] = m;
      lds_arrive(&L->cnt[C_MAX]);
      lds_wait(&L->cnt[C_MAX], 4);
      const v4f m4 = *(LAS v4f*)L->mw;
      const float ml = fmaxf(fmaxf(m4.x, m4.y), fmaxf(m4.z, m4.w)) * 1.4426950408889634f;
      float ls = 0.f;
#pragma unroll
      for (int t = 0; t < 8; ++t) {
        const float pv = __builtin_amdgcn_exp2f(fmaf(S[t], 1.4426950408889634f, -ml));
        ls += pv;
        S[t] = __uint_as_float(pack2(pv, pv - rbf(pv)));
      }
      ls = wave_sum(ls);
      if (lane == 0) L->lw[w] = ls;
      lds_arrive(&L->cnt[C_SUM]);
    }
    v4f acc = {0.f, 0.f, 0.f, 0.f};
    v4u sel;
    {
      const u32 pick = (c & 8) ? 0x0302u : 0x0100u, own = (c & 1) ? (pick << 16) | 0x0c0cu : 0x0c0c0000u | pick;
#pragma unroll
      for (int d = 0; d < 4; ++d) sel[d] = ((c & 7) >> 1) == d ? own : 0x0c0c0c0cu;
    }
#pragma unroll 1
    for (int it = 0; it < 4; ++it) {
      Unroll<0, 8>::run([&](auto J) {
        constexpr int j = J, g = 32 + LAG + j;
        v4u bm[4];
        Unroll<0, 4>::run([&](auto K) { bm[K] = pv_diag<4 * (j & 3) + K>(__float_as_uint(S[j >> 2]), sel); pin(bm[K]); });
        step4<4 * LAG, 4 * (7 - j)>(a + 4 * slot_of(g), a + 4 * slot_of(32 + j), rv, vo, (LAG + j + 8 * it) * 65536, t0 + due(g + 8 * it), j + LAG < 8 ? 1 : 3 - it);
#pragma unroll
        for (int k = 0; k < 4; ++k)
          acc = __builtin_amdgcn_mfma_f32_16x16x32_bf16(__builtin_bit_cast(bf16x8, a[4 * slot_of(32 + j) + k]), __builtin_bit_cast(bf16x8, bm[k]), acc, 0, 0, 0);
        pin(acc);
        __builtin_amdgcn_sched_barrier(0);
      });
#pragma unroll
      for (int t = 0; t < 6; ++t) S[t] = S[t + 2];
    }
    LAS float* o = &L->op[w][32 * r + 2 * (c & 8) + (c & 7)];
    const float o0 = acc.x + dppf<ROR8>(acc.x), o1 = acc.y + dppf<ROR8>(acc.y), o2 = acc.z + dppf<ROR8>(acc.z), o3 = acc.w + dppf<ROR8>(acc.w);
    o[0] = (c & 8) ? o2 : o0; o[8] = (c & 8) ? o3 : o1;
    wg_barrier();
    return;
  }

  const int w = wave, t = w * 64 + lane;
  const rsrc_t rs = mkrs(p.ws, WS_END), rm = mkrs(p.wmod, 512 * 3072 * 2);
  const bool first = l >= 16 && w >= 2;
  v2u c0 = {0u, 0u}, c1 = c0;
  v4u W[32];
  if (first) {
    const int row = 64 * xl + 32 * (w - 2) + 4 * (lane >> 3);
    c0 = ((const GAS v2u*)p.c)[row >> 2]; c1 = ((const GAS v2u*)p.c)[128 + (row >> 2)];
#pragma unroll
    for (int k = 0; k < 4; ++k) W[k] = ldw(rm, row * 6144 + (lane & 7) * 16, k * 6144 + (l - 16) * 128);
  }
  const u32 epoch = __hip_atomic_load((GAS u32*)(p.ws + WS_EPOCH), __ATOMIC_RELAXED, __HIP_MEMORY_SCOPE_AGENT);
  const int j8 = 2 * s + (w & 1);
  v4u xs = {0u, 0u, 0u, 0u};
  u32 xo = 0;
  if (w < 2) {
    xs = ((const GAS v4u*)p.x)[b * 64 + lane];
    xo = ((const GAS u32*)p.x)[b * 256 + 32 * j8 + 8 * r + (c & 7)];
    const rsrc_t rq = mkrs(p.wq, 512 * 4096 * 2);
#pragma unroll
    for (int k = 0; k < 16; ++k) W[k] = ldw(rq, (64 * j8 + 16 * r) * 8192 + h * 256 + c * 16, k * 8192);
  }
  __builtin_amdgcn_sched_barrier(0);
  if (threadIdx.x < 16) L->cnt[threadIdx.x] = 0;
  wg_barrier();
  __builtin_amdgcn_sched_barrier(0);
  if (w == 3 && l == 12) {
    u64 pc;
    asm volatile("s_getpc_b64 %0" : "=s"(pc));
#pragma unroll
    for (int k = 0; k < CODE_KB; ++k) W[k] = ((const GAS v4u*)(pc & ~127ull))[k * 64 + lane];
#pragma unroll
    for (int k = 0; k < CODE_KB; ++k) pin(W[k]);
  }
  const u32 tag = ((u32)__builtin_amdgcn_readfirstlane(epoch) | 1u) + 2u;
  if (l == 0 && w == 3) publish(rs, lane == 0 ? WS_T + xl * 16 : OOB, t0, t0, tag);
  if (first) {
    float v[16] = {0.f, 0.f, 0.f, 0.f, 0.f, 0.f, 0.f, 0.f, 0.f, 0.f, 0.f, 0.f, 0.f, 0.f, 0.f, 0.f};
    rows2(W[0], W[1], silu2(c0.x), silu2(c1.x), v);
    rows2(W[2], W[3], silu2(c0.y), silu2(c1.y), v);
#pragma unroll
    for (int j = 0; j < 8; ++j) v[j] = sum32(v[j], v[j + 8]);
#pragma unroll
    for (int j = 0; j < 4; ++j) v[j] = sum16(v[j], v[j + 4]);
    L->red1[w][lane] = v2f{trstep<ROR8>(v[0], v[2], lane & 8), trstep<ROR8>(v[1], v[3], lane & 8)};
    if (last_of(&L->cnt[C_MP], 2, lane)) {
      const int src = (lane & 32) + (lane & 3) * 8 + ((lane >> 2) & 7);
      publish(rs, WS_MP + ((l - 16) * 8 + xl) * 1024 + lane * 16, L->red1[2][src] + L->red1[3][src], tag);
    }
  }
  if (w < 2) {
    v2f mean, rstd;
    {
      v2f e[8];
#pragma unroll
      for (int k = 0; k < 4; ++k) { e[2 * k] = v2f{bflo(xs[k]), bflo(xs[k])}; e[2 * k + 1] = v2f{bfhi(xs[k]), bfhi(xs[k])}; }
      ln_stats(e, lane, mean, rstd);
      pin(mean); pin(rstd);
    }
#pragma unroll
    for (int k = 0; k < 8; ++k) { interleave(W[2 * k], W[2 * k + 1]); pin(W[2 * k]); pin(W[2 * k + 1]); }
    u32 off[8];
#pragma unroll
    for (int k = 0; k < 8; ++k) off[k] = WS_MP + (((c & 8) + j8) * 8 + k) * 1024 + b * 512 + (8 * r + (c & 7)) * 16;
    v4u val[8];
    hold(t0 + MOD_T);
    gather<8, 2>(rs, off, val, tag, t0);
    v2f m = payload(val[0]);
#pragma unroll
    for (int k = 1; k < 8; ++k) m += payload(val[k]);
    const u32 own = pack2(m.x, m.y), oth = dppu<ROR8>(own);
    const v2f hv = modulate(unpack2(xo), mean, rstd, rbf2(unpack2((c & 8) ? own : oth) + 1.f), unpack2((c & 8) ? oth : own));
    float u[4];
    gemv<8>(W, pack2(hv.x, hv.y), u);
    L->red1[w][lane] = v2f{sum16(u[0], u[2]), sum16(u[1], u[3])};
    if (last_of(&L->cnt[C_QP], 2, lane)) {
      publish(rs, WS_QP + (bh * 4 + s) * 1024 + lane * 16, L->red1[0][lane] + L->red1[1][lane], tag);
      lds_arrive(&L->cnt[C_QPUB]);
    }
  } else if (w == 2) {
    const u32 o0 = WS_QP + bh * 4096 + lane * 16, off[5] = {o0, o0 + 1024, o0 + 2048, o0 + 3072, WS_T + (lane & 7) * 16};
    v4u val[5];
    lds_wait(&L->cnt[C_QPUB], 1);
    __builtin_amdgcn_s_sleep(D_Q);
    gather<5, 2>(rs, off, val, tag, t0);
    int d = (int)(val[4].x - t0);
    d = min(d, (int)dppu<XOR1>(d)); d = min(d, (int)dppu<XOR2>(d)); d = min(d, (int)dppu<HMIR>(d));
    L->cnt[C_SHIFT] = max(min(d, 0), -200);
    const v2f qv = (payload(val[0]) + payload(val[1])) + (payload(val[2]) + payload(val[3]));
    L->qd[4 * c + r] = pack2(qv.x, qv.y);
    lds_arrive(&L->cnt[C_Q]);
  }

  const int fstart = (43 * xl + 3) >> 2, fg = fstart + l;
  const bool ffn = fg < (43 * (xl + 1) + 3) >> 2;
  v4u F[8];
  v4u W2[4];
#pragma unroll
  for (int k = 0; k < 8; ++k) F[k] = v4u{0u, 0u, 0u, 0u};
#pragma unroll
  for (int k = 0; k < 4; ++k) W2[k] = v4u{0u, 0u, 0u, 0u};
  v4u Wo[8];
  hold(t0 + LATE_T);
#pragma unroll
  for (int k = 0; k < 8; ++k) Wo[k] = ldw(mkrs(p.wo, 4096 * 512 * 2), (h * 128 + 32 * s + 2 * r) * 1024 + w * 256 + c * 16, (k / 2) * 8192 + (k & 1) * 1024);
  __builtin_amdgcn_sched_barrier(0);
  if (l >= 24) {
    const int rg = xl * 8 + l - 24, row = 128 * w + 8 * (lane >> 2);
    const v4u ca = ((const GAS v4u*)p.c)[row >> 3], cb = ((const GAS v4u*)p.c)[64 + (row >> 3)];
#pragma unroll
    for (int k = 0; k < 8; ++k) W[k] = ldw(rm, row * 6144 + (lane & 3) * 16, k * 6144 + 2048 + rg * 64);
    float v[16] = {0.f, 0.f, 0.f, 0.f, 0.f, 0.f, 0.f, 0.f, 0.f, 0.f, 0.f, 0.f, 0.f, 0.f, 0.f, 0.f};
#pragma unroll
    for (int k = 0; k < 4; ++k) rows2(W[2 * k], W[2 * k + 1], silu2(ca[k]), silu2(cb[k]), v);
#pragma unroll
    for (int j = 0; j < 8; ++j) v[j] = sum32(v[j], v[j + 8]);
#pragma unroll
    for (int j = 0; j < 4; ++j) { v[j] = sum16(v[j], v[j + 4]); v[j] += dppf<ROR8>(v[j]); v[j] += dppf<ROR4>(v[j]); }
    if ((lane & 12) == 0) L->red1b[w][4 * r + (lane & 3)] = v4f{v[0], v[1], v[2], v[3]};
    if (last_of(&L->cnt[C_MOD], 4, lane)) {
      const int src = (c & 8) + (c & 1) * 4 + ((c >> 1) & 3);
      const v4f a = (L->red1b[0][src] + L->red1b[1][src]) + (L->red1b[2][src] + L->red1b[3][src]);
      publish(rs, lane < 16 ? WS_MOD + (c >> 3) * 8192 + (8 * rg + (c & 7)) * 16 : OOB, pack2(a.x, a.y), pack2(a.z, a.w), tag);
    }
  } else if (ffn) {
    const rsrc_t r1 = mkrs(p.w1, 512 * 1376 * 2), r3 = mkrs(p.w3, 512 * 1376 * 2), r2 = mkrs(p.w2, 1376 * 512 * 2);
#pragma unroll
    for (int k = 0; k < 8; ++k) F[k] = ldw(k < 4 ? r1 : r3, (128 * w + (lane >> 1)) * 2752 + (lane & 1) * 16, (k & 3) * 32 * 2752 + fg * 32);
#pragma unroll
    for (int k = 0; k < 4; ++k) W2[k] = ldw(r2, r * 4096 + w * 256 + c * 16, (16 * fg + k) * 1024);
    LAS v4s* tr = (LAS v4s*)&L->stage[w][0] + (8 * r + (c >> 2)) * 4 + (c & 3);
#pragma unroll
    for (int k = 0; k < 8; ++k) {
      L->stage[w][lane] = F[k];
      asm volatile("" ::: "memory");
      const v2u lo = __builtin_bit_cast(v2u, __builtin_amdgcn_ds_read_tr16_b64_v4i16(tr));
      const v2u hi = __builtin_bit_cast(v2u, __builtin_amdgcn_ds_read_tr16_b64_v4i16(tr + 16));
      F[k] = v4u{lo.x, lo.y, hi.x, hi.y};
      asm volatile("" : "+v"(F[k]) :: "memory");
    }
    interleave(W2[0], W2[1]); interleave(W2[2], W2[3]);
  }
#pragma unroll
  for (int k = 0; k < 4; ++k) { interleave(Wo[2 * k], Wo[2 * k + 1]); pin(Wo[2 * k]); pin(Wo[2 * k + 1]); }

  const bool red = l < 4;
  const int j = xl * 4 + (l & 3), rp = 64 * (j >> 3) + 16 * (j & 3) + 4 * (lane & 3) + 2 * ((j >> 2) & 1) + ((lane >> 2) & 1);
  const int tp = w * 64 + 16 * ((lane >> 3) & 3) + 4 * (lane & 3) + 2 * (lane >> 5) + ((lane >> 2) & 1);
  v2f sc0 = {0.f, 0.f}, sc1 = sc0, sh0 = sc0, sh1 = sc0;
  float xr = 0.f, gar = 0.f, gbr = 0.f;
  if (ffn) {
    const u32 modb = WS_MOD + (1 + (lane >> 5)) * 2048 + (32 * w + (lane & 31)) * 16;
    const u32 base = WS_MOD + (r >> 1) * 8192 + (rp >> 1) * 16;
    const u32 off[4] = {modb, modb + 8192, (red && w == 0) ? base : OOB, (red && w == 0) ? base + 3 * 2048 : OOB};
    v4u val[4];
    hold(t0 + MOD2_T);
    const u32 xv = ((const GAS u32*)p.x)[(r >> 1) * 256 + rp];
    gather<4, 16, 2>(rs, off, val, tag, t0);
    auto fetch = [&](int from, const v4u& u) {
      u32 lo = __builtin_amdgcn_ds_bpermute(from, u.x), hi = __builtin_amdgcn_ds_bpermute(from, u.z);
      pin(lo); pin(hi);
      return (tp & 1) ? hi : lo;
    };
    const int src = (tp & 62) * 2;
    const u32 h0 = fetch(src, val[0]), h1 = fetch(src, val[1]), g0 = fetch(src + 128, val[0]), g1 = fetch(src + 128, val[1]);
    sc0 = rbf2(v2f{bflo(g0), bflo(g1)} + 1.f); sc1 = rbf2(v2f{bfhi(g0), bfhi(g1)} + 1.f);
    sh0 = v2f{bflo(h0), bflo(h1)}; sh1 = v2f{bfhi(h0), bfhi(h1)};
    const u32 ga = (rp & 1) ? val[2].z : val[2].x, gb = (rp & 1) ? val[3].z : val[3].x;
    xr = (r & 1) ? bfhi(xv) : bflo(xv); gar = (r & 1) ? bfhi(ga) : bflo(ga); gbr = (r & 1) ? bfhi(gb) : bflo(gb);
  }
  float scale = 0.f;
  if (w == 0) {
    lds_wait(&L->cnt[C_SUM], 4);
    const v4f m4 = *(LAS v4f*)L->mw, l4 = *(LAS v4f*)L->lw;
    const float m = fmaxf(fmaxf(m4.x, m4.y), fmaxf(m4.z, m4.w));
    publish(rs, lane == 0 ? WS_ML + (bh * 4 + s) * 16 : OOB, v2f{m, (l4.x + l4.y) + (l4.z + l4.w)}, tag);
    const u32 off[1] = {WS_ML + (bh * 4 + (lane & 3)) * 16};
    v4u val[1];
    __builtin_amdgcn_s_sleep(D_ML);
    gather<1, 16>(rs, off, val, tag, t0);
    const v2f ml = payload(val[0]);
    float M = fmaxf(ml.x, dppf<XOR1>(ml.x)); M = fmaxf(M, dppf<XOR2>(M));
    float Ls = ml.y * __builtin_amdgcn_exp2f((ml.x - M) * 1.4426950408889634f);
    Ls += dppf<XOR1>(Ls); Ls += dppf<XOR2>(Ls);
    scale = __builtin_amdgcn_exp2f((m - M) * 1.4426950408889634f) / Ls;
  }

  u32 o_h = WS_O + (bh * 4 + s) * 1024 + lane * 16, o_hg[1] = {WS_O + (bh * 4 + (c >> 2)) * 1024 + (16 * s + 4 * r + (c & 3)) * 16};
  u32 o_y = (r & 1) ? OOB : WS_Y + ((8 * w + 2 * r + (c >> 2)) * 256 + bh * 4 + s) * 64 + (c & 3) * 16;
  u32 o_yg = WS_Y + j * 16384 + w * 4096 + lane * 16;
  u32 o_x = (lane >> 3) < REP_X1 ? WS_X1 + (lane >> 3) * 4096 + j * 128 + (lane & 7) * 16 : OOB;
  u32 o_xg[4];
#pragma unroll
  for (int k = 0; k < 4; ++k) { o_xg[k] = WS_X1 + (xl & (REP_X1 - 1)) * 4096 + lane * 16 + ((w + k) & 3) * 1024; pin(o_xg[k]); }
  u32 o_o = WS_OP + ((8 * w + 4 * (r >> 1) + (c >> 2)) * 96 + fg) * 128 + (r & 1) * 64 + (c & 3) * 16;
  u32 o_og[3];
  o_og[0] = WS_OP + j * 12288 + w * 3072 + lane * 16;
  o_og[1] = (w < 3 || (lane >> 3) < NFFN - 80) ? o_og[0] + 1024 : OOB;
  o_og[2] = w < 3 ? o_og[0] + 2048 : OOB;
  u32 o_e = (j == 0 && lane == 0) ? WS_EPOCH : OOB;
  u32 o_out = (lane & 8) == 0 ? ((r >> 1) * 512 + 2 * rp + (r & 1)) * 2 : OOB;
  const rsrc_t rout = mkrs(p.out, 2048);
  LAS v2f* a_o = (LAS v2f*)&L->op[0][32 * r + 8 * (c & 3) + 2 * (c >> 2)];
  LAS float* a_rw = &L->ry[w][4 * r + (lane & 3)];
  LAS float* a_rr = &L->ry[r & 2][2 * (lane & 4) + 4 * (r & 1) + (lane & 3)];
  LAS u32* a_hw = &L->h2[0][tp];
  LAS v4u* a_hr = (LAS v4u*)&L->h2[c & 1][64 * w + 4 * r];
  LAS float* a_gw = &L->red2[lane][w];
  LAS v4f* a_gr = (LAS v4f*)L->red2[(c & 4) * 4 + 4 * r + (c & 3)];
  LAS float* a_ow = &L->ro[w][8 * r + (lane & 7)];
  LAS float* a_or = &L->ro[0][8 * r + (lane & 7)];
  pin(o_h); pin(o_hg[0]); pin(o_y); pin(o_yg); pin(o_x); pin(o_o); pin(o_og[0]); pin(o_og[1]); pin(o_og[2]); pin(o_e); pin(o_out);
  pin(scale); pin(sc0); pin(sc1); pin(sh0); pin(sh1); pin(xr); pin(gar); pin(gbr);
  pin(a_o); pin(a_rw); pin(a_rr); pin(a_hw); pin(a_hr); pin(a_gw); pin(a_gr); pin(a_ow); pin(a_or);

  wg_barrier();
  {
    if (w == 0) publish(rs, o_h, ((a_o[0] + a_o[64]) + (a_o[128] + a_o[192])) * scale, tag);
    v4u val[1];
    __builtin_amdgcn_s_sleep(D_H);
    gather<1, 0>(rs, o_hg, val, tag, t0);
    float o0 = __uint_as_float(val[0].x), o1 = __uint_as_float(val[0].z);
    o0 += dppf<ROR4>(o0); o1 += dppf<ROR4>(o1); o0 += dppf<ROR8>(o0); o1 += dppf<ROR8>(o1);
    float u[4];
    gemv<4>(Wo, pack2(o0, o1), u);
    publish(rs, o_y, pack2(sum16(u[0], u[0]), sum16(u[1], u[1])), pack2(sum16(u[2], u[2]), sum16(u[3], u[3])), tag);
  }
  if (ffn) {
    float x1r = 0.f;
    {
      const u32 off[4] = {o_yg, o_yg + 1024, o_yg + 2048, o_yg + 3072};
      v4u val[4];
      __builtin_amdgcn_s_sleep(D_Y);
      gather<4, 0>(rs, off, val, tag, t0);
      const v2f lo = (unpack2(val[0].x) + unpack2(val[1].x)) + (unpack2(val[2].x) + unpack2(val[3].x));
      const v2f hi = (unpack2(val[0].z) + unpack2(val[1].z)) + (unpack2(val[2].z) + unpack2(val[3].z));
      float a = sum16(sum32(lo.x, hi.x), sum32(lo.y, hi.y));
      a += dppf<ROR8>(a); a += dppf<ROR4>(a);
      a_rw[0] = a;
      wg_barrier();
      if (red && w == 0) {
        x1r = rbf(xr + rbf(gar * rbf(a_rr[0] + a_rr[16])));
        const auto e = __builtin_amdgcn_permlane16_swap(__float_as_uint(x1r), __float_as_uint(x1r), false, false);
        const u32 pr = (e[0] >> 16) | e[1];
        const auto bb = __builtin_amdgcn_permlane32_swap(pr, pr, false, false);
        publish(rs, o_x, bb[0], bb[1], tag);
        __builtin_amdgcn_raw_buffer_store_b32(tag, rs, o_e, 0, AUX_SC1);
        __builtin_amdgcn_s_sleep(D_XR);
      } else __builtin_amdgcn_s_sleep(D_X);
    }
    v4u val[4];
    gather<4, 0>(rs, o_xg, val, tag, t0);
    v2f e[8], mean, rstd;
#pragma unroll
    for (int k = 0; k < 4; ++k) { e[2 * k] = v2f{bflo(val[k].x), bflo(val[k].z)}; e[2 * k + 1] = v2f{bfhi(val[k].x), bfhi(val[k].z)}; }
    ln_stats(e, lane, mean, rstd);
    const v2f ha = modulate(e[0], mean, rstd, sc0, sh0), hb = modulate(e[1], mean, rstd, sc1, sh1);
    a_hw[0] = pack2(ha.x, hb.x); a_hw[256] = pack2(ha.y, hb.y);
    asm volatile("" ::: "memory");
    v4u A[4];
#pragma unroll
    for (int k = 0; k < 4; ++k) A[k] = a_hr[4 * k];
    __builtin_amdgcn_sched_barrier(0);
    v4f dg = {0.f, 0.f, 0.f, 0.f}, du = dg;
#pragma unroll
    for (int k = 0; k < 4; ++k) {
      dg = __builtin_amdgcn_mfma_f32_16x16x32_bf16(__builtin_bit_cast(bf16x8, A[k]), __builtin_bit_cast(bf16x8, F[k]), dg, 0, 0, 0);
      du = __builtin_amdgcn_mfma_f32_16x16x32_bf16(__builtin_bit_cast(bf16x8, A[k]), __builtin_bit_cast(bf16x8, F[4 + k]), du, 0, 0, 0);
    }
    const float lo = (lane & 16) ? dg.y : dg.x, hi = (lane & 16) ? du.y : du.x;
    a_gw[0] = (lane & 32) ? hi : lo;
    wg_barrier();
    const v4f g4 = a_gr[0], u4 = a_gr[32];
    const v2f gu = rbf2(v2f{(g4.x + g4.y) + (g4.z + g4.w), (u4.x + u4.y) + (u4.z + u4.w)});
    const float hh = rbf(silu(gu.x)) * gu.y;
    u32 hp = pack2(hh, dppf<XOR1>(hh));
    float o[16] = {0.f, 0.f, 0.f, 0.f, 0.f, 0.f, 0.f, 0.f, 0.f, 0.f, 0.f, 0.f, 0.f, 0.f, 0.f, 0.f};
    dpp_ready(hp);
    dot8<0>(W2[0], W2[1], hp, o); dot8<4>(W2[0], W2[1], hp, o + 8); dot8<2>(W2[2], W2[3], hp, o); dot8<6>(W2[2], W2[3], hp, o + 8);
    dots_done(o); dots_done(o + 8);
#pragma unroll
    for (int k = 0; k < 4; ++k) { o[k] = sum32(o[k], o[k + 4]); o[8 + k] = sum32(o[8 + k], o[12 + k]); }
    publish(rs, o_o, pack2(sum16(o[0], o[2]), sum16(o[1], o[3])), pack2(sum16(o[8], o[10]), sum16(o[9], o[11])), tag);
    if (red) {
      v4u va[3];
      __builtin_amdgcn_s_sleep(D_O);
      gather<3, 0, 1>(rs, o_og, va, tag, t0);
      const v2f b0 = unpack2(va[0].x) + unpack2(va[1].x) + unpack2(va[2].x), b1 = unpack2(va[0].z) + unpack2(va[1].z) + unpack2(va[2].z);
      float a = sum16(sum32(b0.x, b1.x), sum32(b0.y, b1.y));
      a += dppf<ROR8>(a);
      a_ow[0] = a;
      wg_barrier();
      if (w == 0) {
        const float res = x1r + rbf(gbr * rbf((a_or[0] + a_or[32]) + (a_or[64] + a_or[96])));
        __builtin_amdgcn_raw_buffer_store_b16((u16)pack2(res, res), rout, o_out, 0, 0);
      }
    }
  }
}

extern "C" void kernel_launch(void* const* d_in, const int* in_sizes, int n_in, void* d_out, int out_size, void* d_ws, size_t ws_size, hipStream_t stream) {
  Params p;
  p.x = (const u16*)d_in[0]; p.K = (const u16*)d_in[1]; p.V = (const u16*)d_in[2]; p.wq = (const u16*)d_in[3]; p.wo = (const u16*)d_in[4];
  p.w1 = (const u16*)d_in[6]; p.w3 = (const u16*)d_in[7]; p.w2 = (const u16*)d_in[8]; p.c = (const u16*)d_in[9]; p.wmod = (const u16*)d_in[10];
  p.out = (u16*)d_out; p.ws = (unsigned char*)d_ws;
  hipLaunchKernelGGL(fused_block, dim3(256), dim3(512), 0, stream, p);
}
```

```cpp
#include <hip/hip_runtime.h>
#include <stdint.h>
#include <type_traits>

typedef unsigned short u16;
typedef unsigned int u32;
typedef unsigned long long u64;
typedef u32 v4u __attribute__((ext_vector_type(4)));
typedef u32 v2u __attribute__((ext_vector_type(2)));
typedef float v4f __attribute__((ext_vector_type(4)));
typedef float v2f __attribute__((ext_vector_type(2)));
typedef __bf16 bf16x2 __attribute__((ext_vector_type(2)));
typedef __bf16 bf16x8 __attribute__((ext_vector_type(8)));
typedef short v4s __attribute__((ext_vector_type(4)));
typedef __amdgpu_buffer_rsrc_t rsrc_t;
#define GAS __attribute__((address_space(1)))
#define LAS __attribute__((address_space(3)))

constexpr int KSTART = 100;
constexpr int PACE = 950;
constexpr int DEPTH = 8, DEPTH_G = 10;
constexpr int LAG = 7;
constexpr int PAUSE_G = 12, PAUSE_F = 370, PAUSE_R = 200, CATCH_G = 60;
constexpr int MOD_T = 320, LATE_T = 830, MOD2_T = 1600;
constexpr int D_Q = 30, D_ML = 60, D_H = 15, D_Y = 16, D_X = 19, D_XR = 16, D_O = 16;
constexpr u32 SPIN_TICKS = 400000;
constexpr int CODE_KB = 27;
constexpr int slot_of(int g) { return g < 20 ? g - 8 : (g - 20) & 7; }
constexpr int used_after(int g) { return (g <= 21 || g - LAG < 11) ? 11 : g - LAG; }

constexpr int NFFN = 86, REP_X1 = 4;
constexpr u32 WS_EPOCH = 0;
constexpr u32 WS_MP = 256;
constexpr u32 WS_MOD = WS_MP + 16 * 8 * 1024;
constexpr u32 WS_ML = WS_MOD + 2 * 8192;
constexpr u32 WS_T = WS_ML + 4096;
constexpr u32 WS_X1 = WS_T + 128;
constexpr u32 WS_QP = WS_X1 + REP_X1 * 4096;
constexpr u32 WS_O = WS_QP + 256 * 1024;
constexpr u32 WS_Y = WS_O + 256 * 1024;
constexpr u32 WS_OP = WS_Y + 32 * 256 * 64;
constexpr u32 WS_END = WS_OP + 32 * 96 * 128;
constexpr u32 OOB = 0x7ffffff0u;
constexpr int AUX_SC1 = 16, AUX_NT = 2;

struct Params {
  const u16 *x, *K, *V, *wq, *wo, *w1, *w3, *w2, *c, *wmod;
  u16* out;
  unsigned char* ws;
};

enum { C_MP, C_Q, C_MAX, C_SUM, C_MOD, C_QP, C_QPUB, C_SHIFT };
struct Lds {
  u32 cnt[16];
  v2f red1[4][64];
  v4f red1b[4][16];
  u32 qd[64];
  float mw[4], lw[4];
  float op[4][128];
  float red2[64][4];
  u32 h2[2][256];
  float ry[4][16], ro[4][32];
  v4u stage[4][64];
  v4u kst[4][32][64];
};

__device__ __forceinline__ float bflo(u32 v) { return __uint_as_float(v << 16); }
__device__ __forceinline__ float bfhi(u32 v) { return __uint_as_float(v & 0xffff0000u); }
__device__ __forceinline__ v2f unpack2(u32 v) { return v2f{bflo(v), bfhi(v)}; }
__device__ __forceinline__ u32 pack2(float a, float b) { bf16x2 r = {(__bf16)a, (__bf16)b}; return __builtin_bit_cast(u32, r); }
__device__ __forceinline__ float rbf(float a) { return bflo(pack2(a, a)); }
__device__ __forceinline__ v2f rbf2(v2f v) { u32 r = pack2(v.x, v.y); asm("" : "+v"(r)); return unpack2(r); }
__device__ __forceinline__ float dot2(u32 a, u32 b, float acc) {
  return __builtin_amdgcn_fdot2_f32_bf16(__builtin_bit_cast(bf16x2, a), __builtin_bit_cast(bf16x2, b), acc, false);
}
__device__ __forceinline__ float silu(float v) { return v * __builtin_amdgcn_rcpf(1.f + __expf(-v)); }
__device__ __forceinline__ u32 silu2(u32 v) { return pack2(silu(bflo(v)), silu(bfhi(v))); }
template <int CTRL> __device__ __forceinline__ u32 dppu(u32 x) { return (u32)__builtin_amdgcn_mov_dpp((int)x, CTRL, 0xf, 0xf, true); }
template <int CTRL> __device__ __forceinline__ float dppf(float x) { return __uint_as_float(dppu<CTRL>(__float_as_uint(x))); }
constexpr int XOR1 = 0xB1, XOR2 = 0x4E, HMIR = 0x141, ROR4 = 0x124, ROR8 = 0x128, BCAST = 0x150;
__device__ __forceinline__ float sum32(float a, float b) {
  auto s = __builtin_amdgcn_permlane32_swap(__float_as_uint(a), __float_as_uint(b), false, false);
  return __uint_as_float(s[0]) + __uint_as_float(s[1]);
}
__device__ __forceinline__ float sum16(float a, float b) {
  auto s = __builtin_amdgcn_permlane16_swap(__float_as_uint(a), __float_as_uint(b), false, false);
  return __uint_as_float(s[0]) + __uint_as_float(s[1]);
}
template <int CTRL> __device__ __forceinline__ float trstep(float lo, float hi, bool bit) {
  const float a = lo + dppf<CTRL>(lo), b = hi + dppf<CTRL>(hi);
  return bit ? b : a;
}
__device__ __forceinline__ float wave_sum(float x) {
  x = sum32(x, x); x = sum16(x, x);
  x += dppf<ROR8>(x); x += dppf<HMIR>(x); x += dppf<XOR2>(x); x += dppf<XOR1>(x);
  return x;
}
__device__ __forceinline__ float wave_max(float x) {
  x = fmaxf(x, dppf<XOR1>(x)); x = fmaxf(x, dppf<XOR2>(x)); x = fmaxf(x, dppf<HMIR>(x)); x = fmaxf(x, dppf<ROR8>(x));
  auto s = __builtin_amdgcn_permlane16_swap(__float_as_uint(x), __float_as_uint(x), false, false);
  x = fmaxf(__uint_as_float(s[0]), __uint_as_float(s[1]));
  auto t = __builtin_amdgcn_permlane32_swap(__float_as_uint(x), __float_as_uint(x), false, false);
  return fmaxf(__uint_as_float(t[0]), __uint_as_float(t[1]));
}
__device__ __forceinline__ rsrc_t mkrs(const void* p, u32 bytes) { return __builtin_amdgcn_make_buffer_rsrc((void*)p, 0, bytes, 0x00020000); }
__device__ __forceinline__ v4u ldw(rsrc_t rs, u32 voff, u32 soff) { return __builtin_amdgcn_raw_buffer_load_b128(rs, voff, soff, AUX_NT); }
__device__ __forceinline__ u32 now() { return (u32)__builtin_amdgcn_s_memrealtime(); }
template <class T> __device__ __forceinline__ void pin(T& v) { asm volatile("" : "+v"(v)); }
template <int I, int E> struct Unroll {
  template <class F> static __device__ __forceinline__ void run(F&& f) {
    if constexpr (I < E) { f(std::integral_constant<int, I>{}); Unroll<I + 1, E>::run(f); }
  }
};

__device__ __forceinline__ void lds_arrive(LAS u32* c) {
  asm volatile("s_mov_b64 exec, 1\n\tds_add_u32 %0, %1\n\ts_mov_b64 exec, -1" :: "v"((u32)(uintptr_t)c), "v"(1u) : "memory");
}
__device__ __forceinline__ void lds_wait(LAS u32* c, u32 n) {
  while ((u32)__builtin_amdgcn_readfirstlane(__hip_atomic_load(c, __ATOMIC_RELAXED, __HIP_MEMORY_SCOPE_WORKGROUP)) < n) __builtin_amdgcn_s_sleep(1);
  asm volatile("" ::: "memory");
}
__device__ __forceinline__ bool last_of(LAS u32* c, u32 n, int lane) {
  asm volatile("" ::: "memory");
  u32 old = 0;
  if (lane == 0) old = __hip_atomic_fetch_add(c, 1u, __ATOMIC_RELAXED, __HIP_MEMORY_SCOPE_WORKGROUP);
  const bool last = (u32)__builtin_amdgcn_readfirstlane(old) == n - 1u;
  asm volatile("" ::: "memory");
  return last;
}
__device__ __forceinline__ void wg_barrier() { asm volatile("s_waitcnt lgkmcnt(0)\n\ts_barrier" ::: "memory"); }

__device__ __forceinline__ void publish(rsrc_t rs, u32 off, u32 a, u32 b, u32 tag) {
  __builtin_amdgcn_raw_buffer_store_b128(v4u{a, tag, b, tag}, rs, off, 0, AUX_SC1);
}
__device__ __forceinline__ void publish(rsrc_t rs, u32 off, v2f v, u32 tag) { publish(rs, off, __float_as_uint(v.x), __float_as_uint(v.y), tag); }
template <int N, int SLEEP, int NFULL = N> __device__ __forceinline__ void gather(rsrc_t rs, const u32 (&off)[N], v4u (&val)[N], u32 tag, u32 t0) {
  for (;;) {
#pragma unroll
    for (int k = 0; k < N; ++k) val[k] = __builtin_amdgcn_raw_buffer_load_b128(rs, off[k], 0, AUX_SC1);
    u32 bad = 0u;
#pragma unroll
    for (int k = 0; k < N; ++k) {
      const u32 m = (val[k].y ^ tag) | (val[k].w ^ tag);
      bad |= (k >= NFULL && off[k] == OOB) ? 0u : m;
    }
    asm("" : "+v"(bad));
    if (__builtin_expect(!__any(bad != 0u), 1) || now() - t0 > SPIN_TICKS) break;
    if (SLEEP) __builtin_amdgcn_s_sleep(SLEEP);
  }
}
__device__ __forceinline__ v2f payload(const v4u& u) { return v2f{__uint_as_float(u.x), __uint_as_float(u.z)}; }

__device__ __forceinline__ void interleave(v4u& a, v4u& b) {
  const v4u lo = {__builtin_amdgcn_perm(b.x, a.x, 0x05040100u), __builtin_amdgcn_perm(b.x, a.x, 0x07060302u),
                  __builtin_amdgcn_perm(b.y, a.y, 0x05040100u), __builtin_amdgcn_perm(b.y, a.y, 0x07060302u)};
  const v4u hi = {__builtin_amdgcn_perm(b.z, a.z, 0x05040100u), __builtin_amdgcn_perm(b.z, a.z, 0x07060302u),
                  __builtin_amdgcn_perm(b.w, a.w, 0x05040100u), __builtin_amdgcn_perm(b.w, a.w, 0x07060302u)};
  a = lo; b = hi;
}
__device__ __forceinline__ void rows2(v4u a, v4u b, u32 s0, u32 s1, float (&v)[16]) {
  interleave(a, b);
#pragma unroll
  for (int j = 0; j < 4; ++j) {
    v[j] = dot2(a[j], s0, v[j]); v[4 + j] = dot2(b[j], s0, v[4 + j]);
    v[8 + j] = dot2(a[j], s1, v[8 + j]); v[12 + j] = dot2(b[j], s1, v[12 + j]);
  }
}
template <int LANE> __device__ __forceinline__ void dot8(const v4u& wa, const v4u& wb, u32 vp, float* a) {
#define DOT(i, j) "v_dot2c_f32_bf16_dpp %" #i ", %8, %" #j " row_newbcast:%c17 row_mask:0xf bank_mask:0xf\n\t"
  asm(DOT(0, 9) DOT(1, 10) DOT(2, 11) DOT(3, 12) DOT(4, 13) DOT(5, 14) DOT(6, 15) DOT(7, 16)
      : "+v"(a[0]), "+v"(a[1]), "+v"(a[2]), "+v"(a[3]), "+v"(a[4]), "+v"(a[5]), "+v"(a[6]), "+v"(a[7])
      : "v"(vp), "v"(wa.x), "v"(wa.y), "v"(wa.z), "v"(wa.w), "v"(wb.x), "v"(wb.y), "v"(wb.z), "v"(wb.w), "i"(LANE));
#undef DOT
}
__device__ __forceinline__ void dpp_ready(u32& vp) { asm("s_nop 1" : "+v"(vp)); }
__device__ __forceinline__ void dots_done(float* a) {
  asm("s_nop 2" : "+v"(a[0]), "+v"(a[1]), "+v"(a[2]), "+v"(a[3]), "+v"(a[4]), "+v"(a[5]), "+v"(a[6]), "+v"(a[7]));
}
template <int NK> __device__ __forceinline__ void gemv(const v4u* W, u32 vp, float (&u)[4]) {
  float a[8] = {0.f, 0.f, 0.f, 0.f, 0.f, 0.f, 0.f, 0.f};
  dpp_ready(vp);
  Unroll<0, NK>::run([&](auto K) { dot8<K>(W[2 * K], W[2 * K + 1], vp, a); });
  dots_done(a);
#pragma unroll
  for (int j = 0; j < 4; ++j) u[j] = sum32(a[j], a[j + 4]);
}

__device__ __forceinline__ void ln_stats(const v2f (&e)[8], int lane, v2f& mean, v2f& rstd) {
  const v2f piv = {__uint_as_float(__builtin_amdgcn_readlane(__float_as_uint(e[0].x), 0)), __uint_as_float(__builtin_amdgcn_readlane(__float_as_uint(e[0].y), 0))};
  v2f d[8];
#pragma unroll
  for (int j = 0; j < 8; ++j) d[j] = e[j] - piv;
  const v2f s = ((d[0] + d[1]) + (d[2] + d[3])) + ((d[4] + d[5]) + (d[6] + d[7]));
  v2f qa = d[0] * d[0], qb = d[1] * d[1];
#pragma unroll
  for (int j = 2; j < 8; j += 2) { qa += d[j] * d[j]; qb += d[j + 1] * d[j + 1]; }
  qa += qb;
  float a = sum16(sum32(s.x, s.y), sum32(qa.x, qa.y));
  a += dppf<ROR8>(a); a += dppf<HMIR>(a); a += dppf<XOR2>(a); a += dppf<XOR1>(a);
  const auto t = __builtin_amdgcn_permlane16_swap(__float_as_uint(a), __float_as_uint(a), false, false);
  const float md = __uint_as_float(t[0]) * (1.f / 512.f), var = fmaxf(fmaf(-md, md, __uint_as_float(t[1]) * (1.f / 512.f)), 0.f);
  const v2f mv = rbf2(v2f{md + (lane < 32 ? piv.x : piv.y), var});
  const float rs = __builtin_amdgcn_rcpf(rbf(__builtin_amdgcn_sqrtf(rbf(mv.y + 1e-5f))));
  const auto m2 = __builtin_amdgcn_permlane32_swap(__float_as_uint(mv.x), __float_as_uint(mv.x), false, false);
  const auto r2 = __builtin_amdgcn_permlane32_swap(__float_as_uint(rs), __float_as_uint(rs), false, false);
  mean = v2f{__uint_as_float(m2[0]), __uint_as_float(m2[1])};
  rstd = v2f{__uint_as_float(r2[0]), __uint_as_float(r2[1])};
}
__device__ __forceinline__ v2f modulate(v2f x, v2f mean, v2f rstd, v2f scale1, v2f shift) { return rbf2(rbf2(rbf2(x - mean) * rstd) * scale1) + shift; }

__device__ __forceinline__ float qk(const v4u k, const v4u q) { return dot2(k.w, q.w, dot2(k.z, q.z, dot2(k.y, q.y, dot2(k.x, q.x, 0.f)))); }
__device__ __forceinline__ float tile_scores(float (&x)[16], int lane) {
#pragma unroll
  for (int j = 0; j < 8; ++j) x[j] = trstep<ROR8>(x[j], x[j + 8], lane & 8);
#pragma unroll
  for (int j = 0; j < 4; ++j) x[j] = trstep<HMIR>(x[j], x[j + 4], lane & 4);
#pragma unroll
  for (int j = 0; j < 2; ++j) x[j] = trstep<XOR2>(x[j], x[j + 2], lane & 2);
  const float d = trstep<XOR1>(x[0], x[1], lane & 1);
  return rbf(rbf(d) * 0.08838834764831845f);
}
template <int J> __device__ __forceinline__ v4u pv_diag(u32 phl, const v4u& sel) {
  const u32 t = dppu<BCAST + J>(phl);
  return v4u{__builtin_amdgcn_perm(t, t, sel.x), __builtin_amdgcn_perm(t, t, sel.y), __builtin_amdgcn_perm(t, t, sel.z), __builtin_amdgcn_perm(t, t, sel.w)};
}
#define HOLD(T) "pace%=:\n\ts_memrealtime s[40:41]\n\ts_waitcnt lgkmcnt(0)\n\ts_sub_u32 s40, s40, " T "\n\ts_cmp_ge_i32 s40, 0\n\t" \
                "s_cbranch_scc1 paced%=\n\ts_sleep 1\n\ts_branch pace%=\npaced%=:\n\t"
__device__ __forceinline__ void hold(u32 t) { asm volatile(HOLD("%0") :: "s"(t) : "s40", "s41", "scc"); }
__device__ __forceinline__ void dma4(rsrc_t rs, const v4u& vo, u32 soff, u32 lds) {
  u32 keep;
  asm volatile(
      "s_mov_b32 %0, m0\n\ts_mov_b32 m0, %7\n\ts_nop 0\n\t"
      "buffer_load_dwordx4 %1, %5, %6 offen nt lds\n\tbuffer_load_dwordx4 %2, %5, %6 offen offset:1024 nt lds\n\t"
      "buffer_load_dwordx4 %3, %5, %6 offen offset:2048 nt lds\n\tbuffer_load_dwordx4 %4, %5, %6 offen offset:3072 nt lds\n\t"
      "s_mov_b32 m0, %0"
      : "=&s"(keep) : "v"(vo.x), "v"(vo.y), "v"(vo.z), "v"(vo.w), "s"(rs), "s"(soff), "s"(lds) : "memory");
}
#define LOAD4(A, B, C, D, RS, SO) \
  "buffer_load_dwordx4 %0, " A ", " RS ", " SO " offen nt\n\tbuffer_load_dwordx4 %1, " B ", " RS ", " SO " offen offset:1024 nt\n\t" \
  "buffer_load_dwordx4 %2, " C ", " RS ", " SO " offen offset:2048 nt\n\tbuffer_load_dwordx4 %3, " D ", " RS ", " SO " offen offset:3072 nt\n\t"
__device__ __forceinline__ void load4_first(v4u* d, rsrc_t rs, const v4u& vo, u32 soff) {
  asm volatile(LOAD4("%4", "%5", "%6", "%7", "%8", "%9")
               : "=&v"(d[0]), "=&v"(d[1]), "=&v"(d[2]), "=&v"(d[3]) : "v"(vo.x), "v"(vo.y), "v"(vo.z), "v"(vo.w), "s"(rs), "s"(soff));
}
__device__ __forceinline__ void load4(v4u* d, rsrc_t rs, const v4u& vo, u32 soff) {
  asm volatile(LOAD4("%4", "%5", "%6", "%7", "%8", "%9")
               : "+v"(d[0]), "+v"(d[1]), "+v"(d[2]), "+v"(d[3]) : "v"(vo.x), "v"(vo.y), "v"(vo.z), "v"(vo.w), "s"(rs), "s"(soff));
}
template <int N> __device__ __forceinline__ void landed(v4u* d) {
  asm volatile("s_waitcnt vmcnt(%4)" : "+v"(d[0]), "+v"(d[1]), "+v"(d[2]), "+v"(d[3]) : "i"(N));
}
template <int NL, int ND> __device__ __forceinline__ void step4(v4u* d, v4u* u, rsrc_t rs, const v4u& vo, u32 soff, u32 t, u32 live) {
  asm volatile(
      "s_cmp_eq_u32 %15, 0\n\ts_cbranch_scc1 dead%=\n" HOLD("%14") LOAD4("%8", "%9", "%10", "%11", "%12", "%13")
      "s_waitcnt vmcnt(%16)\n\ts_branch done%=\n"
      "dead%=:\n\ts_waitcnt vmcnt(%17)\n"
      "done%=:"
      : "+v"(d[0]), "+v"(d[1]), "+v"(d[2]), "+v"(d[3]), "+v"(u[0]), "+v"(u[1]), "+v"(u[2]), "+v"(u[3])
      : "v"(vo.x), "v"(vo.y), "v"(vo.z), "v"(vo.w), "s"(rs), "s"(soff), "s"(t), "s"(live), "i"(NL), "i"(ND) : "s40", "s41", "scc");
}

__global__ void __launch_bounds__(512, 2) fused_block(Params p) {
  __shared__ __attribute__((aligned(16))) Lds L_;
  LAS Lds* L = (LAS Lds*)&L_;
  const int lane = threadIdx.x & 63, wave = __builtin_amdgcn_readfirstlane(threadIdx.x >> 6);
  const int r = lane >> 4, c = lane & 15;
  const int xl = blockIdx.x & 7, l = blockIdx.x >> 3;
  const int bh = (l >> 2) * 8 + xl, b = bh >> 5, h = bh & 31, s = l & 3;
  u32 t0 = now();

  if (wave >= 4) {
    const int w = wave - 4;
    const rsrc_t rk = mkrs(p.K + (size_t)bh * (8192 * 128), 8192 * 256), rv = mkrs(p.V + (size_t)bh * (8192 * 128), 8192 * 256);
    const u32 v0 = s * 16384 + w * 1024 + lane * 16;
    const v4u vo = {v0, v0 + 3072, v0 + 2 * 3072, v0 + 3 * 3072};
    const int pause = l < 12 ? PAUSE_F : l >= 24 ? PAUSE_R : 0, fast = PACE - pause * 16 / (CATCH_G - PAUSE_G);
    const int resume = KSTART + ((PAUSE_G * PACE) >> 4) + pause;
    auto due = [&](int g) { const int d = KSTART + ((g * PACE) >> 4); return g < PAUSE_G ? d : max(d, resume + (((g - PAUSE_G) * fast) >> 4)); };
    v4u a[48];
    auto issue = [&](auto G) {
      constexpr int g = G;
      hold(t0 + due(g));
      if constexpr (g < 20) asm volatile("s_waitcnt vmcnt(%0)" :: "i"(g < DEPTH_G ? DEPTH - 4 : 28));
      if constexpr (g < 8) dma4(rk, vo, g * 65536, (u32)(uintptr_t)&L->kst[w][4 * g][0]);
      else if constexpr (g < 20) load4_first(a + 4 * slot_of(g), rk, vo, g * 65536);
      else load4(a + 4 * slot_of(g), g < 32 ? rk : rv, vo, (g & 31) * 65536);
      __builtin_amdgcn_sched_barrier(0);
    };
    __builtin_amdgcn_s_barrier();
    Unroll<0, 20>::run(issue);
    lds_wait(&L->cnt[C_Q], 1);
    t0 += __builtin_amdgcn_readfirstlane(L->cnt[C_SHIFT]);
    const v4u q = *(LAS v4u*)&L->qd[4 * c];
    float S[8], x[16];
    auto use = [&](auto G, auto LAST) {
      constexpr int g = G;
      if constexpr (g >= 8) landed<4 * (LAST - g)>(a + 4 * slot_of(g));
#pragma unroll
      for (int k = 0; k < 4; ++k) {
        if constexpr (g < 8) x[4 * (g & 3) + k] = qk(L->kst[w][4 * g + k][lane], q);
        else x[4 * (g & 3) + k] = qk(a[4 * slot_of(g) + k], q);
      }
      if constexpr ((g & 3) == 3) S[g >> 2] = tile_scores(x, lane);
      __builtin_amdgcn_sched_barrier(0);
    };
    Unroll<8, 12>::run([&](auto U) { use(U, std::integral_constant<int, 19>{}); });
    asm volatile("" ::: "memory");
    issue(std::integral_constant<int, 20>{});
    Unroll<0, 4>::run([&](auto U) { use(U, std::integral_constant<int, 20>{}); });
    issue(std::integral_constant<int, 21>{});
    Unroll<4, 8>::run([&](auto U) { use(U, std::integral_constant<int, 21>{}); });
    Unroll<22, 32 + LAG>::run([&](auto G) {
      issue(G);
      Unroll<used_after(G - 1) + 1, used_after(G) + 1>::run([&](auto U) { use(U, G); });
    });
    {
      float m = wave_max(fmaxf(fmaxf(fmaxf(S[0], S[1]), fmaxf(S[2], S[3])), fmaxf(fmaxf(S[4], S[5]), fmaxf(S[6], S[7]))));
      if (lane == 0) L->mw[w] = m;
      lds_arrive(&L->cnt[C_MAX]);
      lds_wait(&L->cnt[C_MAX], 4);
      const v4f m4 = *(LAS v4f*)L->mw;
      const float ml = fmaxf(fmaxf(m4.x, m4.y), fmaxf(m4.z, m4.w)) * 1.4426950408889634f;
      float ls = 0.f;
#pragma unroll
      for (int t = 0; t < 8; ++t) {
        const float pv = __builtin_amdgcn_exp2f(fmaf(S[t], 1.4426950408889634f, -ml));
        ls += pv;
        S[t] = __uint_as_float(pack2(pv, pv - rbf(pv)));
      }
      ls = wave_sum(ls);
      if (lane == 0) L->lw[w] = ls;
      lds_arrive(&L->cnt[C_SUM]);
    }
    v4f acc = {0.f, 0.f, 0.f, 0.f};
    v4u sel;
    {
      const u32 pick = (c & 8) ? 0x0302u : 0x0100u, own = (c & 1) ? (pick << 16) | 0x0c0cu : 0x0c0c0000u | pick;
#pragma unroll
      for (int d = 0; d < 4; ++d) sel[d] = ((c & 7) >> 1) == d ? own : 0x0c0c0c0cu;
    }
#pragma unroll 1
    for (int it = 0; it < 4; ++it) {
      Unroll<0, 8>::run([&](auto J) {
        constexpr int j = J, g = 32 + LAG + j;
        v4u bm[4];
        Unroll<0, 4>::run([&](auto K) { bm[K] = pv_diag<4 * (j & 3) + K>(__float_as_uint(S[j >> 2]), sel); pin(bm[K]); });
        step4<4 * LAG, 4 * (7 - j)>(a + 4 * slot_of(g), a + 4 * slot_of(32 + j), rv, vo, (LAG + j + 8 * it) * 65536, t0 + due(g + 8 * it), j + LAG < 8 ? 1 : 3 - it);
#pragma unroll
        for (int k = 0; k < 4; ++k)
          acc = __builtin_amdgcn_mfma_f32_16x16x32_bf16(__builtin_bit_cast(bf16x8, a[4 * slot_of(32 + j) + k]), __builtin_bit_cast(bf16x8, bm[k]), acc, 0, 0, 0);
        pin(acc);
        __builtin_amdgcn_sched_barrier(0);
      });
#pragma unroll
      for (int t = 0; t < 6; ++t) S[t] = S[t + 2];
    }
    LAS float* o = &L->op[w][32 * r + 2 * (c & 8) + (c & 7)];
    const float o0 = acc.x + dppf<ROR8>(acc.x), o1 = acc.y + dppf<ROR8>(acc.y), o2 = acc.z + dppf<ROR8>(acc.z), o3 = acc.w + dppf<ROR8>(acc.w);
    o[0] = (c & 8) ? o2 : o0; o[8] = (c & 8) ? o3 : o1;
    wg_barrier();
    return;
  }

  const int w = wave, t = w * 64 + lane;
  const rsrc_t rs = mkrs(p.ws, WS_END), rm = mkrs(p.wmod, 512 * 3072 * 2);
  const bool first = l >= 16 && w >= 2;
  v2u c0 = {0u, 0u}, c1 = c0;
  v4u W[32];
  if (first) {
    const int row = 64 * xl + 32 * (w - 2) + 4 * (lane >> 3);
    c0 = ((const GAS v2u*)p.c)[row >> 2]; c1 = ((const GAS v2u*)p.c)[128 + (row >> 2)];
#pragma unroll
    for (int k = 0; k < 4; ++k) W[k] = ldw(rm, row * 6144 + (lane & 7) * 16, k * 6144 + (l - 16) * 128);
  }
  const u32 epoch = __hip_atomic_load((GAS u32*)(p.ws + WS_EPOCH), __ATOMIC_RELAXED, __HIP_MEMORY_SCOPE_AGENT);
  const int j8 = 2 * s + (w & 1);
  v4u xs = {0u, 0u, 0u, 0u};
  u32 xo = 0;
  if (w < 2) {
    xs = ((const GAS v4u*)p.x)[b * 64 + lane];
    xo = ((const GAS u32*)p.x)[b * 256 + 32 * j8 + 8 * r + (c & 7)];
    const rsrc_t rq = mkrs(p.wq, 512 * 4096 * 2);
#pragma unroll
    for (int k = 0; k < 16; ++k) W[k] = ldw(rq, (64 * j8 + 16 * r) * 8192 + h * 256 + c * 16, k * 8192);
  }
  __builtin_amdgcn_sched_barrier(0);
  if (threadIdx.x < 16) L->cnt[threadIdx.x] = 0;
  wg_barrier();
  __builtin_amdgcn_sched_barrier(0);
  if (w == 3 && l == 12) {
    u64 pc;
    asm volatile("s_getpc_b64 %0" : "=s"(pc));
#pragma unroll
    for (int k = 0; k < CODE_KB; ++k) W[k] = ((const GAS v4u*)(pc & ~127ull))[k * 64 + lane];
#pragma unroll
    for (int k = 0; k < CODE_KB; ++k) pin(W[k]);
  }
  const u32 tag = ((u32)__builtin_amdgcn_readfirstlane(epoch) | 1u) + 2u;
  if (l == 0 && w == 3) publish(rs, lane == 0 ? WS_T + xl * 16 : OOB, t0, t0, tag);
  if (first) {
    float v[16] = {0.f, 0.f, 0.f, 0.f, 0.f, 0.f, 0.f, 0.f, 0.f, 0.f, 0.f, 0.f, 0.f, 0.f, 0.f, 0.f};
    rows2(W[0], W[1], silu2(c0.x), silu2(c1.x), v);
    rows2(W[2], W[3], silu2(c0.y), silu2(c1.y), v);
#pragma unroll
    for (int j = 0; j < 8; ++j) v[j] = sum32(v[j], v[j + 8]);
#pragma unroll
    for (int j = 0; j < 4; ++j) v[j] = sum16(v[j], v[j + 4]);
    L->red1[w][lane] = v2f{trstep<ROR8>(v[0], v[2], lane & 8), trstep<ROR8>(v[1], v[3], lane & 8)};
    if (last_of(&L->cnt[C_MP], 2, lane)) {
      const int src = (lane & 32) + (lane & 3) * 8 + ((lane >> 2) & 7);
      publish(rs, WS_MP + ((l - 16) * 8 + xl) * 1024 + lane * 16, L->red1[2][src] + L->red1[3][src], tag);
    }
  }
  if (w < 2) {
    v2f mean, rstd;
    {
      v2f e[8];
#pragma unroll
      for (int k = 0; k < 4; ++k) { e[2 * k] = v2f{bflo(xs[k]), bflo(xs[k])}; e[2 * k + 1] = v2f{bfhi(xs[k]), bfhi(xs[k])}; }
      ln_stats(e, lane, mean, rstd);
      pin(mean); pin(rstd);
    }
#pragma unroll
    for (int k = 0; k < 8; ++k) { interleave(W[2 * k], W[2 * k + 1]); pin(W[2 * k]); pin(W[2 * k + 1]); }
    u32 off[8];
#pragma unroll
    for (int k = 0; k < 8; ++k) off[k] = WS_MP + (((c & 8) + j8) * 8 + k) * 1024 + b * 512 + (8 * r + (c & 7)) * 16;
    v4u val[8];
    hold(t0 + MOD_T);
    gather<8, 2>(rs, off, val, tag, t0);
    v2f m = payload(val[0]);
#pragma unroll
    for (int k = 1; k < 8; ++k) m += payload(val[k]);
    const u32 own = pack2(m.x, m.y), oth = dppu<ROR8>(own);
    const v2f hv = modulate(unpack2(xo), mean, rstd, rbf2(unpack2((c & 8) ? own : oth) + 1.f), unpack2((c & 8) ? oth : own));
    float u[4];
    gemv<8>(W, pack2(hv.x, hv.y), u);
    L->red1[w][lane] = v2f{sum16(u[0], u[2]), sum16(u[1], u[3])};
    if (last_of(&L->cnt[C_QP], 2, lane)) {
      publish(rs, WS_QP + (bh * 4 + s) * 1024 + lane * 16, L->red1[0][lane] + L->red1[1][lane], tag);
      lds_arrive(&L->cnt[C_QPUB]);
    }
  } else if (w == 2) {
    const u32 o0 = WS_QP + bh * 4096 + lane * 16, off[5] = {o0, o0 + 1024, o0 + 2048, o0 + 3072, WS_T + (lane & 7) * 16};
    v4u val[5];
    lds_wait(&L->cnt[C_QPUB], 1);
    __builtin_amdgcn_s_sleep(D_Q);
    gather<5, 2>(rs, off, val, tag, t0);
    int d = (int)(val[4].x - t0);
    d = min(d, (int)dppu<XOR1>(d)); d = min(d, (int)dppu<XOR2>(d)); d = min(d, (int)dppu<HMIR>(d));
    L->cnt[C_SHIFT] = max(min(d, 0), -200);
    const v2f qv = (payload(val[0]) + payload(val[1])) + (payload(val[2]) + payload(val[3]));
    L->qd[4 * c + r] = pack2(qv.x, qv.y);
    lds_arrive(&L->cnt[C_Q]);
  }

  const int fstart = (43 * xl + 3) >> 2, fg = fstart + l;
  const bool ffn = fg < (43 * (xl + 1) + 3) >> 2;
  v4u F[8];
  v4u W2[4];
#pragma unroll
  for (int k = 0; k < 8; ++k) F[k] = v4u{0u, 0u, 0u, 0u};
#pragma unroll
  for (int k = 0; k < 4; ++k) W2[k] = v4u{0u, 0u, 0u, 0u};
  v4u Wo[8];
  hold(t0 + LATE_T);
#pragma unroll
  for (int k = 0; k < 8; ++k) Wo[k] = ldw(mkrs(p.wo, 4096 * 512 * 2), (h * 128 + 32 * s + 2 * r) * 1024 + w * 256 + c * 16, (k / 2) * 8192 + (k & 1) * 1024);
  __builtin_amdgcn_sched_barrier(0);
  if (l >= 24) {
    const int rg = xl * 8 + l - 24, row = 128 * w + 8 * (lane >> 2);
    const v4u ca = ((const GAS v4u*)p.c)[row >> 3], cb = ((const GAS v4u*)p.c)[64 + (row >> 3)];
#pragma unroll
    for (int k = 0; k < 8; ++k) W[k] = ldw(rm, row * 6144 + (lane & 3) * 16, k * 6144 + 2048 + rg * 64);
    float v[16] = {0.f, 0.f, 0.f, 0.f, 0.f, 0.f, 0.f, 0.f, 0.f, 0.f, 0.f, 0.f, 0.f, 0.f, 0.f, 0.f};
#pragma unroll
    for (int k = 0; k < 4; ++k) rows2(W[2 * k], W[2 * k + 1], silu2(ca[k]), silu2(cb[k]), v);
#pragma unroll
    for (int j = 0; j < 8; ++j) v[j] = sum32(v[j], v[j + 8]);
#pragma unroll
    for (int j = 0; j < 4; ++j) { v[j] = sum16(v[j], v[j + 4]); v[j] += dppf<ROR8>(v[j]); v[j] += dppf<ROR4>(v[j]); }
    if ((lane & 12) == 0) L->red1b[w][4 * r + (lane & 3)] = v4f{v[0], v[1], v[2], v[3]};
    if (last_of(&L->cnt[C_MOD], 4, lane)) {
      const int src = (c & 8) + (c & 1) * 4 + ((c >> 1) & 3);
      const v4f a = (L->red1b[0][src] + L->red1b[1][src]) + (L->red1b[2][src] + L->red1b[3][src]);
      publish(rs, lane < 16 ? WS_MOD + (c >> 3) * 8192 + (8 * rg + (c & 7)) * 16 : OOB, pack2(a.x, a.y), pack2(a.z, a.w), tag);
    }
  } else if (ffn) {
    const rsrc_t r1 = mkrs(p.w1, 512 * 1376 * 2), r3 = mkrs(p.w3, 512 * 1376 * 2), r2 = mkrs(p.w2, 1376 * 512 * 2);
#pragma unroll
    for (int k = 0; k < 8; ++k) F[k] = ldw(k < 4 ? r1 : r3, (128 * w + (lane >> 1)) * 2752 + (lane & 1) * 16, (k & 3) * 32 * 2752 + fg * 32);
#pragma unroll
    for (int k = 0; k < 4; ++k) W2[k] = ldw(r2, r * 4096 + w * 256 + c * 16, (16 * fg + k) * 1024);
    LAS v4s* tr = (LAS v4s*)&L->stage[w][0] + (8 * r + (c >> 2)) * 4 + (c & 3);
#pragma unroll
    for (int k = 0; k < 8; ++k) {
      L->stage[w][lane] = F[k];
      asm volatile("" ::: "memory");
      const v2u lo = __builtin_bit_cast(v2u, __builtin_amdgcn_ds_read_tr16_b64_v4i16(tr));
      const v2u hi = __builtin_bit_cast(v2u, __builtin_amdgcn_ds_read_tr16_b64_v4i16(tr + 16));
      F[k] = v4u{lo.x, lo.y, hi.x, hi.y};
      asm volatile("" : "+v"(F[k]) :: "memory");
    }
    interleave(W2[0], W2[1]); interleave(W2[2], W2[3]);
  }
#pragma unroll
  for (int k = 0; k < 4; ++k) { interleave(Wo[2 * k], Wo[2 * k + 1]); pin(Wo[2 * k]); pin(Wo[2 * k + 1]); }

  const bool red = l < 4;
  const int j = xl * 4 + (l & 3), rp = 64 * (j >> 3) + 16 * (j & 3) + 4 * (lane & 3) + 2 * ((j >> 2) & 1) + ((lane >> 2) & 1);
  const int tp = w * 64 + 16 * ((lane >> 3) & 3) + 4 * (lane & 3) + 2 * (lane >> 5) + ((lane >> 2) & 1);
  v2f sc0 = {0.f, 0.f}, sc1 = sc0, sh0 = sc0, sh1 = sc0;
  float xr = 0.f, gar = 0.f, gbr = 0.f;
  if (ffn) {
    const u32 modb = WS_MOD + (1 + (lane >> 5)) * 2048 + (32 * w + (lane & 31)) * 16;
    const u32 base = WS_MOD + (r >> 1) * 8192 + (rp >> 1) * 16;
    const u32 off[4] = {modb, modb + 8192, (red && w == 0) ? base : OOB, (red && w == 0) ? base + 3 * 2048 : OOB};
    v4u val[4];
    hold(t0 + MOD2_T);
    const u32 xv = ((const GAS u32*)p.x)[(r >> 1) * 256 + rp];
    gather<4, 16, 2>(rs, off, val, tag, t0);
    auto fetch = [&](int from, const v4u& u) {
      u32 lo = __builtin_amdgcn_ds_bpermute(from, u.x), hi = __builtin_amdgcn_ds_bpermute(from, u.z);
      pin(lo); pin(hi);
      return (tp & 1) ? hi : lo;
    };
    const int src = (tp & 62) * 2;
    const u32 h0 = fetch(src, val[0]), h1 = fetch(src, val[1]), g0 = fetch(src + 128, val[0]), g1 = fetch(src + 128, val[1]);
    sc0 = rbf2(v2f{bflo(g0), bflo(g1)} + 1.f); sc1 = rbf2(v2f{bfhi(g0), bfhi(g1)} + 1.f);
    sh0 = v2f{bflo(h0), bflo(h1)}; sh1 = v2f{bfhi(h0), bfhi(h1)};
    const u32 ga = (rp & 1) ? val[2].z : val[2].x, gb = (rp & 1) ? val[3].z : val[3].x;
    xr = (r & 1) ? bfhi(xv) : bflo(xv); gar = (r & 1) ? bfhi(ga) : bflo(ga); gbr = (r & 1) ? bfhi(gb) : bflo(gb);
  }
  float scale = 0.f;
  if (w == 0) {
    lds_wait(&L->cnt[C_SUM], 4);
    const v4f m4 = *(LAS v4f*)L->mw, l4 = *(LAS v4f*)L->lw;
    const float m = fmaxf(fmaxf(m4.x, m4.y), fmaxf(m4.z, m4.w));
    publish(rs, lane == 0 ? WS_ML + (bh * 4 + s) * 16 : OOB, v2f{m, (l4.x + l4.y) + (l4.z + l4.w)}, tag);
    const u32 off[1] = {WS_ML + (bh * 4 + (lane & 3)) * 16};
    v4u val[1];
    __builtin_amdgcn_s_sleep(D_ML);
    gather<1, 16>(rs, off, val, tag, t0);
    const v2f ml = payload(val[0]);
    float M = fmaxf(ml.x, dppf<XOR1>(ml.x)); M = fmaxf(M, dppf<XOR2>(M));
    float Ls = ml.y * __builtin_amdgcn_exp2f((ml.x - M) * 1.4426950408889634f);
    Ls += dppf<XOR1>(Ls); Ls += dppf<XOR2>(Ls);
    scale = __builtin_amdgcn_exp2f((m - M) * 1.4426950408889634f) / Ls;
  }

  u32 o_h = WS_O + (bh * 4 + s) * 1024 + lane * 16, o_hg[1] = {WS_O + (bh * 4 + (c >> 2)) * 1024 + (16 * s + 4 * r + (c & 3)) * 16};
  u32 o_y = (r & 1) ? OOB : WS_Y + ((8 * w + 2 * r + (c >> 2)) * 256 + bh * 4 + s) * 64 + (c & 3) * 16;
  u32 o_yg = WS_Y + j * 16384 + w * 4096 + lane * 16;
  u32 o_x = (lane >> 3) < REP_X1 ? WS_X1 + (lane >> 3) * 4096 + j * 128 + (lane & 7) * 16 : OOB;
  u32 o_xg[4];
#pragma unroll
  for (int k = 0; k < 4; ++k) { o_xg[k] = WS_X1 + (xl & (REP_X1 - 1)) * 4096 + lane * 16 + ((w + k) & 3) * 1024; pin(o_xg[k]); }
  u32 o_o = WS_OP + ((8 * w + 4 * (r >> 1) + (c >> 2)) * 96 + fg) * 128 + (r & 1) * 64 + (c & 3) * 16;
  u32 o_og[3];
  o_og[0] = WS_OP + j * 12288 + w * 3072 + lane * 16;
  o_og[1] = (w < 3 || (lane >> 3) < NFFN - 80) ? o_og[0] + 1024 : OOB;
  o_og[2] = w < 3 ? o_og[0] + 2048 : OOB;
  u32 o_e = (j == 0 && lane == 0) ? WS_EPOCH : OOB;
  u32 o_out = (lane & 8) == 0 ? ((r >> 1) * 512 + 2 * rp + (r & 1)) * 2 : OOB;
  const rsrc_t rout = mkrs(p.out, 2048);
  LAS v2f* a_o = (LAS v2f*)&L->op[0][32 * r + 8 * (c & 3) + 2 * (c >> 2)];
  LAS float* a_rw = &L->ry[w][4 * r + (lane & 3)];
  LAS float* a_rr = &L->ry[r & 2][2 * (lane & 4) + 4 * (r & 1) + (lane & 3)];
  LAS u32* a_hw = &L->h2[0][tp];
  LAS v4u* a_hr = (LAS v4u*)&L->h2[c & 1][64 * w + 4 * r];
  LAS float* a_gw = &L->red2[lane][w];
  LAS v4f* a_gr = (LAS v4f*)L->red2[(c & 4) * 4 + 4 * r + (c & 3)];
  LAS float* a_ow = &L->ro[w][8 * r + (lane & 7)];
  LAS float* a_or = &L->ro[0][8 * r + (lane & 7)];
  pin(o_h); pin(o_hg[0]); pin(o_y); pin(o_yg); pin(o_x); pin(o_o); pin(o_og[0]); pin(o_og[1]); pin(o_og[2]); pin(o_e); pin(o_out);
  pin(scale); pin(sc0); pin(sc1); pin(sh0); pin(sh1); pin(xr); pin(gar); pin(gbr);
  pin(a_o); pin(a_rw); pin(a_rr); pin(a_hw); pin(a_hr); pin(a_gw); pin(a_gr); pin(a_ow); pin(a_or);

  wg_barrier();
  {
    if (w == 0) publish(rs, o_h, ((a_o[0] + a_o[64]) + (a_o[128] + a_o[192])) * scale, tag);
    v4u val[1];
    __builtin_amdgcn_s_sleep(D_H);
    gather<1, 0>(rs, o_hg, val, tag, t0);
    float o0 = __uint_as_float(val[0].x), o1 = __uint_as_float(val[0].z);
    o0 += dppf<ROR4>(o0); o1 += dppf<ROR4>(o1); o0 += dppf<ROR8>(o0); o1 += dppf<ROR8>(o1);
    float u[4];
    gemv<4>(Wo, pack2(o0, o1), u);
    publish(rs, o_y, pack2(sum16(u[0], u[0]), sum16(u[1], u[1])), pack2(sum16(u[2], u[2]), sum16(u[3], u[3])), tag);
  }
  if (ffn) {
    float x1r = 0.f;
    {
      const u32 off[4] = {o_yg, o_yg + 1024, o_yg + 2048, o_yg + 3072};
      v4u val[4];
      __builtin_amdgcn_s_sleep(D_Y);
      gather<4, 0>(rs, off, val, tag, t0);
      const v2f lo = (unpack2(val[0].x) + unpack2(val[1].x)) + (unpack2(val[2].x) + unpack2(val[3].x));
      const v2f hi = (unpack2(val[0].z) + unpack2(val[1].z)) + (unpack2(val[2].z) + unpack2(val[3].z));
      float a = sum16(sum32(lo.x, hi.x), sum32(lo.y, hi.y));
      a += dppf<ROR8>(a); a += dppf<ROR4>(a);
      a_rw[0] = a;
      wg_barrier();
      if (red && w == 0) {
        x1r = rbf(xr + rbf(gar * rbf(a_rr[0] + a_rr[16])));
        const auto e = __builtin_amdgcn_permlane16_swap(__float_as_uint(x1r), __float_as_uint(x1r), false, false);
        const u32 pr = (e[0] >> 16) | e[1];
        const auto bb = __builtin_amdgcn_permlane32_swap(pr, pr, false, false);
        publish(rs, o_x, bb[0], bb[1], tag);
        __builtin_amdgcn_raw_buffer_store_b32(tag, rs, o_e, 0, AUX_SC1);
        __builtin_amdgcn_s_sleep(D_XR);
      } else __builtin_amdgcn_s_sleep(D_X);
    }
    v4u val[4];
    gather<4, 0>(rs, o_xg, val, tag, t0);
    v2f e[8], mean, rstd;
#pragma unroll
    for (int k = 0; k < 4; ++k) { e[2 * k] = v2f{bflo(val[k].x), bflo(val[k].z)}; e[2 * k + 1] = v2f{bfhi(val[k].x), bfhi(val[k].z)}; }
    ln_stats(e, lane, mean, rstd);
    const v2f ha = modulate(e[0], mean, rstd, sc0, sh0), hb = modulate(e[1], mean, rstd, sc1, sh1);
    a_hw[0] = pack2(ha.x, hb.x); a_hw[256] = pack2(ha.y, hb.y);
    asm volatile("" ::: "memory");
    v4u A[4];
#pragma unroll
    for (int k = 0; k < 4; ++k) A[k] = a_hr[4 * k];
    __builtin_amdgcn_sched_barrier(0);
    v4f dg = {0.f, 0.f, 0.f, 0.f}, du = dg;
#pragma unroll
    for (int k = 0; k < 4; ++k) {
      dg = __builtin_amdgcn_mfma_f32_16x16x32_bf16(__builtin_bit_cast(bf16x8, A[k]), __builtin_bit_cast(bf16x8, F[k]), dg, 0, 0, 0);
      du = __builtin_amdgcn_mfma_f32_16x16x32_bf16(__builtin_bit_cast(bf16x8, A[k]), __builtin_bit_cast(bf16x8, F[4 + k]), du, 0, 0, 0);
    }
    const float lo = (lane & 16) ? dg.y : dg.x, hi = (lane & 16) ? du.y : du.x;
    a_gw[0] = (lane & 32) ? hi : lo;
    wg_barrier();
    const v4f g4 = a_gr[0], u4 = a_gr[32];
    const v2f gu = rbf2(v2f{(g4.x + g4.y) + (g4.z + g4.w), (u4.x + u4.y) + (u4.z + u4.w)});
    const float hh = rbf(silu(gu.x)) * gu.y;
    u32 hp = pack2(hh, dppf<XOR1>(hh));
    float o[16] = {0.f, 0.f, 0.f, 0.f, 0.f, 0.f, 0.f, 0.f, 0.f, 0.f, 0.f, 0.f, 0.f, 0.f, 0.f, 0.f};
    dpp_ready(hp);
    dot8<0>(W2[0], W2[1], hp, o); dot8<4>(W2[0], W2[1], hp, o + 8); dot8<2>(W2[2], W2[3], hp, o); dot8<6>(W2[2], W2[3], hp, o + 8);
    dots_done(o); dots_done(o + 8);
#pragma unroll
    for (int k = 0; k < 4; ++k) { o[k] = sum32(o[k], o[k + 4]); o[8 + k] = sum32(o[8 + k], o[12 + k]); }
    publish(rs, o_o, pack2(sum16(o[0], o[2]), sum16(o[1], o[3])), pack2(sum16(o[8], o[10]), sum16(o[9], o[11])), tag);
    if (red) {
      v4u va[3];
      __builtin_amdgcn_s_sleep(D_O);
      gather<3, 0, 1>(rs, o_og, va, tag, t0);
      const v2f b0 = unpack2(va[0].x) + unpack2(va[1].x) + unpack2(va[2].x), b1 = unpack2(va[0].z) + unpack2(va[1].z) + unpack2(va[2].z);
      float a = sum16(sum32(b0.x, b1.x), sum32(b0.y, b1.y));
      a += dppf<ROR8>(a);
      a_ow[0] = a;
      wg_barrier();
      if (w == 0) {
        const float res = x1r + rbf(gbr * rbf((a_or[0] + a_or[32]) + (a_or[64] + a_or[96])));
        __builtin_amdgcn_raw_buffer_store_b16((u16)pack2(res, res), rout, o_out, 0, 0);
      }
    }
  }
}

extern "C" void kernel_launch(void* const* d_in, const int* in_sizes, int n_in, void* d_out, int out_size, void* d_ws, size_t ws_size, hipStream_t stream) {
  Params p;
  p.x = (const u16*)d_in[0]; p.K = (const u16*)d_in[1]; p.V = (const u16*)d_in[2]; p.wq = (const u16*)d_in[3]; p.wo = (const u16*)d_in[4];
  p.w1 = (const u16*)d_in[6]; p.w3 = (const u16*)d_in[7]; p.w2 = (const u16*)d_in[8]; p.c = (const u16*)d_in[9]; p.wmod = (const u16*)d_in[10];
  p.out = (u16*)d_out; p.ws = (unsigned char*)d_ws;
  hipLaunchKernelGGL(fused_block, dim3(256), dim3(512), 0, stream, p);
}
```

```cpp
#include <hip/hip_runtime.h>
#include <stdint.h>
#include <type_traits>

typedef unsigned short u16;
typedef unsigned int u32;
typedef unsigned long long u64;
typedef u32 v4u __attribute__((ext_vector_type(4)));
typedef u32 v2u __attribute__((ext_vector_type(2)));
typedef float v4f __attribute__((ext_vector_type(4)));
typedef float v2f __attribute__((ext_vector_type(2)));
typedef __bf16 bf16x2 __attribute__((ext_vector_type(2)));
typedef __bf16 bf16x8 __attribute__((ext_vector_type(8)));
typedef short v4s __attribute__((ext_vector_type(4)));
typedef __amdgpu_buffer_rsrc_t rsrc_t;
#define GAS __attribute__((address_space(1)))
#define LAS __attribute__((address_space(3)))

constexpr int KSTART = 100;
constexpr int PACE = 950;
constexpr int DEPTH = 8, DEPTH_G = 12;
constexpr int LAG = 7;
constexpr int PAUSE_G = 14, PAUSE_F = 370, PAUSE_R = 200, FAST = 800;
constexpr int MOD_T = 400, LATE_T = 945, MOD2_T = 1600;
constexpr int D_Q = 30, D_ML = 60, D_H = 15, D_Y = 16, D_X = 19, D_XR = 16, D_O = 16;
constexpr u32 SPIN_TICKS = 400000;
constexpr int CODE_KB = 27;
constexpr int slot_of(int g) { return g < 20 ? g - 8 : (g - 20) & 7; }
constexpr int used_after(int g) { return (g <= 21 || g - LAG < 11) ? 11 : g - LAG; }

constexpr int NFFN = 86, REP_X1 = 4;
constexpr u32 WS_EPOCH = 0;
constexpr u32 WS_MP = 256;
constexpr u32 WS_MOD = WS_MP + 16 * 8 * 1024;
constexpr u32 WS_ML = WS_MOD + 2 * 8192;
constexpr u32 WS_T = WS_ML + 4096;
constexpr u32 WS_X1 = WS_T + 128;
constexpr u32 WS_QP = WS_X1 + REP_X1 * 4096;
constexpr u32 WS_O = WS_QP + 256 * 1024;
constexpr u32 WS_Y = WS_O + 256 * 1024;
constexpr u32 WS_OP = WS_Y + 32 * 256 * 64;
constexpr u32 WS_END = WS_OP + 32 * 96 * 128;
constexpr u32 OOB = 0x7ffffff0u;
constexpr int AUX_SC1 = 16, AUX_NT = 2;

struct Params {
  const u16 *x, *K, *V, *wq, *wo, *w1, *w3, *w2, *c, *wmod;
  u16* out;
  unsigned char* ws;
};

enum { C_MP, C_Q, C_MAX, C_SUM, C_MOD, C_QP, C_QPUB, C_SHIFT };
struct Lds {
  u32 cnt[16];
  v2f red1[4][64];
  v4f red1b[4][16];
  u32 qd[64];
  float mw[4], lw[4];
  float op[4][128];
  float red2[64][4];
  u32 h2[2][256];
  float ry[4][16], ro[4][32];
  v4u stage[4][64];
  v4u kst[4][32][64];
};

__device__ __forceinline__ float bflo(u32 v) { return __uint_as_float(v << 16); }
__device__ __forceinline__ float bfhi(u32 v) { return __uint_as_float(v & 0xffff0000u); }
__device__ __forceinline__ v2f unpack2(u32 v) { return v2f{bflo(v), bfhi(v)}; }
__device__ __forceinline__ u32 pack2(float a, float b) { bf16x2 r = {(__bf16)a, (__bf16)b}; return __builtin_bit_cast(u32, r); }
__device__ __forceinline__ float rbf(float a) { return bflo(pack2(a, a)); }
__device__ __forceinline__ v2f rbf2(v2f v) { u32 r = pack2(v.x, v.y); asm("" : "+v"(r)); return unpack2(r); }
__device__ __forceinline__ float dot2(u32 a, u32 b, float acc) {
  return __builtin_amdgcn_fdot2_f32_bf16(__builtin_bit_cast(bf16x2, a), __builtin_bit_cast(bf16x2, b), acc, false);
}
__device__ __forceinline__ float silu(float v) { return v * __builtin_amdgcn_rcpf(1.f + __expf(-v)); }
__device__ __forceinline__ u32 silu2(u32 v) { return pack2(silu(bflo(v)), silu(bfhi(v))); }
template <int CTRL> __device__ __forceinline__ u32 dppu(u32 x) { return (u32)__builtin_amdgcn_mov_dpp((int)x, CTRL, 0xf, 0xf, true); }
template <int CTRL> __device__ __forceinline__ float dppf(float x) { return __uint_as_float(dppu<CTRL>(__float_as_uint(x))); }
constexpr int XOR1 = 0xB1, XOR2 = 0x4E, HMIR = 0x141, ROR4 = 0x124, ROR8 = 0x128, BCAST = 0x150;
__device__ __forceinline__ float sum32(float a, float b) {
  auto s = __builtin_amdgcn_permlane32_swap(__float_as_uint(a), __float_as_uint(b), false, false);
  return __uint_as_float(s[0]) + __uint_as_float(s[1]);
}
__device__ __forceinline__ float sum16(float a, float b) {
  auto s = __builtin_amdgcn_permlane16_swap(__float_as_uint(a), __float_as_uint(b), false, false);
  return __uint_as_float(s[0]) + __uint_as_float(s[1]);
}
template <int CTRL> __device__ __forceinline__ float trstep(float lo, float hi, bool bit) {
  const float a = lo + dppf<CTRL>(lo), b = hi + dppf<CTRL>(hi);
  return bit ? b : a;
}
__device__ __forceinline__ float wave_sum(float x) {
  x = sum32(x, x); x = sum16(x, x);
  x += dppf<ROR8>(x); x += dppf<HMIR>(x); x += dppf<XOR2>(x); x += dppf<XOR1>(x);
  return x;
}
__device__ __forceinline__ float wave_max(float x) {
  x = fmaxf(x, dppf<XOR1>(x)); x = fmaxf(x, dppf<XOR2>(x)); x = fmaxf(x, dppf<HMIR>(x)); x = fmaxf(x, dppf<ROR8>(x));
  auto s = __builtin_amdgcn_permlane16_swap(__float_as_uint(x), __float_as_uint(x), false, false);
  x = fmaxf(__uint_as_float(s[0]), __uint_as_float(s[1]));
  auto t = __builtin_amdgcn_permlane32_swap(__float_as_uint(x), __float_as_uint(x), false, false);
  return fmaxf(__uint_as_float(t[0]), __uint_as_float(t[1]));
}
__device__ __forceinline__ rsrc_t mkrs(const void* p, u32 bytes) { return __builtin_amdgcn_make_buffer_rsrc((void*)p, 0, bytes, 0x00020000); }
__device__ __forceinline__ v4u ldw(rsrc_t rs, u32 voff, u32 soff) { return __builtin_amdgcn_raw_buffer_load_b128(rs, voff, soff, AUX_NT); }
__device__ __forceinline__ u32 now() { return (u32)__builtin_amdgcn_s_memrealtime(); }
template <class T> __device__ __forceinline__ void pin(T& v) { asm volatile("" : "+v"(v)); }
template <int I, int E> struct Unroll {
  template <class F> static __device__ __forceinline__ void run(F&& f) {
    if constexpr (I < E) { f(std::integral_constant<int, I>{}); Unroll<I + 1, E>::run(f); }
  }
};

__device__ __forceinline__ void lds_arrive(LAS u32* c) {
  asm volatile("s_mov_b64 exec, 1\n\tds_add_u32 %0, %1\n\ts_mov_b64 exec, -1" :: "v"((u32)(uintptr_t)c), "v"(1u) : "memory");
}
__device__ __forceinline__ void lds_wait(LAS u32* c, u32 n) {
  while ((u32)__builtin_amdgcn_readfirstlane(__hip_atomic_load(c, __ATOMIC_RELAXED, __HIP_MEMORY_SCOPE_WORKGROUP)) < n) __builtin_amdgcn_s_sleep(1);
  asm volatile("" ::: "memory");
}
__device__ __forceinline__ bool last_of(LAS u32* c, u32 n, int lane) {
  asm volatile("" ::: "memory");
  u32 old = 0;
  if (lane == 0) old = __hip_atomic_fetch_add(c, 1u, __ATOMIC_RELAXED, __HIP_MEMORY_SCOPE_WORKGROUP);
  const bool last = (u32)__builtin_amdgcn_readfirstlane(old) == n - 1u;
  asm volatile("" ::: "memory");
  return last;
}
__device__ __forceinline__ void wg_barrier() { asm volatile("s_waitcnt lgkmcnt(0)\n\ts_barrier" ::: "memory"); }

__device__ __forceinline__ void publish(rsrc_t rs, u32 off, u32 a, u32 b, u32 tag) {
  __builtin_amdgcn_raw_buffer_store_b128(v4u{a, tag, b, tag}, rs, off, 0, AUX_SC1);
}
__device__ __forceinline__ void publish(rsrc_t rs, u32 off, v2f v, u32 tag) { publish(rs, off, __float_as_uint(v.x), __float_as_uint(v.y), tag); }
template <int N, int SLEEP, int NFULL = N> __device__ __forceinline__ void gather(rsrc_t rs, const u32 (&off)[N], v4u (&val)[N], u32 tag, u32 t0) {
  for (;;) {
#pragma unroll
    for (int k = 0; k < N; ++k) val[k] = __builtin_amdgcn_raw_buffer_load_b128(rs, off[k], 0, AUX_SC1);
    u32 bad = 0u;
#pragma unroll
    for (int k = 0; k < N; ++k) {
      const u32 m = (val[k].y ^ tag) | (val[k].w ^ tag);
      bad |= (k >= NFULL && off[k] == OOB) ? 0u : m;
    }
    asm("" : "+v"(bad));
    if (__builtin_expect(!__any(bad != 0u), 1) || now() - t0 > SPIN_TICKS) break;
    if (SLEEP) __builtin_amdgcn_s_sleep(SLEEP);
  }
}
__device__ __forceinline__ v2f payload(const v4u& u) { return v2f{__uint_as_float(u.x), __uint_as_float(u.z)}; }

__device__ __forceinline__ void interleave(v4u& a, v4u& b) {
  const v4u lo = {__builtin_amdgcn_perm(b.x, a.x, 0x05040100u), __builtin_amdgcn_perm(b.x, a.x, 0x07060302u),
                  __builtin_amdgcn_perm(b.y, a.y, 0x05040100u), __builtin_amdgcn_perm(b.y, a.y, 0x07060302u)};
  const v4u hi = {__builtin_amdgcn_perm(b.z, a.z, 0x05040100u), __builtin_amdgcn_perm(b.z, a.z, 0x07060302u),
                  __builtin_amdgcn_perm(b.w, a.w, 0x05040100u), __builtin_amdgcn_perm(b.w, a.w, 0x07060302u)};
  a = lo; b = hi;
}
__device__ __forceinline__ void rows2(v4u a, v4u b, u32 s0, u32 s1, float (&v)[16]) {
  interleave(a, b);
#pragma unroll
  for (int j = 0; j < 4; ++j) {
    v[j] = dot2(a[j], s0, v[j]); v[4 + j] = dot2(b[j], s0, v[4 + j]);
    v[8 + j] = dot2(a[j], s1, v[8 + j]); v[12 + j] = dot2(b[j], s1, v[12 + j]);
  }
}
template <int LANE> __device__ __forceinline__ void dot8(const v4u& wa, const v4u& wb, u32 vp, float* a) {
#define DOT(i, j) "v_dot2c_f32_bf16_dpp %" #i ", %8, %" #j " row_newbcast:%c17 row_mask:0xf bank_mask:0xf\n\t"
  asm(DOT(0, 9) DOT(1, 10) DOT(2, 11) DOT(3, 12) DOT(4, 13) DOT(5, 14) DOT(6, 15) DOT(7, 16)
      : "+v"(a[0]), "+v"(a[1]), "+v"(a[2]), "+v"(a[3]), "+v"(a[4]), "+v"(a[5]), "+v"(a[6]), "+v"(a[7])
      : "v"(vp), "v"(wa.x), "v"(wa.y), "v"(wa.z), "v"(wa.w), "v"(wb.x), "v"(wb.y), "v"(wb.z), "v"(wb.w), "i"(LANE));
#undef DOT
}
__device__ __forceinline__ void dpp_ready(u32& vp) { asm("s_nop 1" : "+v"(vp)); }
__device__ __forceinline__ void dots_done(float* a) {
  asm("s_nop 2" : "+v"(a[0]), "+v"(a[1]), "+v"(a[2]), "+v"(a[3]), "+v"(a[4]), "+v"(a[5]), "+v"(a[6]), "+v"(a[7]));
}
template <int NK> __device__ __forceinline__ void gemv(const v4u* W, u32 vp, float (&u)[4]) {
  float a[8] = {0.f, 0.f, 0.f, 0.f, 0.f, 0.f, 0.f, 0.f};
  dpp_ready(vp);
  Unroll<0, NK>::run([&](auto K) { dot8<K>(W[2 * K], W[2 * K + 1], vp, a); });
  dots_done(a);
#pragma unroll
  for (int j = 0; j < 4; ++j) u[j] = sum32(a[j], a[j + 4]);
}

__device__ __forceinline__ void ln_stats(const v2f (&e)[8], int lane, v2f& mean, v2f& rstd) {
  const v2f piv = {__uint_as_float(__builtin_amdgcn_readlane(__float_as_uint(e[0].x), 0)), __uint_as_float(__builtin_amdgcn_readlane(__float_as_uint(e[0].y), 0))};
  v2f d[8];
#pragma unroll
  for (int j = 0; j < 8; ++j) d[j] = e[j] - piv;
  const v2f s = ((d[0] + d[1]) + (d[2] + d[3])) + ((d[4] + d[5]) + (d[6] + d[7]));
  v2f qa = d[0] * d[0], qb = d[1] * d[1];
#pragma unroll
  for (int j = 2; j < 8; j += 2) { qa += d[j] * d[j]; qb += d[j + 1] * d[j + 1]; }
  qa += qb;
  float a = sum16(sum32(s.x, s.y), sum32(qa.x, qa.y));
  a += dppf<ROR8>(a); a += dppf<HMIR>(a); a += dppf<XOR2>(a); a += dppf<XOR1>(a);
  const auto t = __builtin_amdgcn_permlane16_swap(__float_as_uint(a), __float_as_uint(a), false, false);
  const float md = __uint_as_float(t[0]) * (1.f / 512.f), var = fmaxf(fmaf(-md, md, __uint_as_float(t[1]) * (1.f / 512.f)), 0.f);
  const v2f mv = rbf2(v2f{md + (lane < 32 ? piv.x : piv.y), var});
  const float rs = __builtin_amdgcn_rcpf(rbf(__builtin_amdgcn_sqrtf(rbf(mv.y + 1e-5f))));
  const auto m2 = __builtin_amdgcn_permlane32_swap(__float_as_uint(mv.x), __float_as_uint(mv.x), false, false);
  const auto r2 = __builtin_amdgcn_permlane32_swap(__float_as_uint(rs), __float_as_uint(rs), false, false);
  mean = v2f{__uint_as_float(m2[0]), __uint_as_float(m2[1])};
  rstd = v2f{__uint_as_float(r2[0]), __uint_as_float(r2[1])};
}
__device__ __forceinline__ v2f modulate(v2f x, v2f mean, v2f rstd, v2f scale1, v2f shift) { return rbf2(rbf2(rbf2(x - mean) * rstd) * scale1) + shift; }

__device__ __forceinline__ float qk(const v4u k, const v4u q) { return dot2(k.w, q.w, dot2(k.z, q.z, dot2(k.y, q.y, dot2(k.x, q.x, 0.f)))); }
__device__ __forceinline__ float tile_scores(float (&x)[16], int lane) {
#pragma unroll
  for (int j = 0; j < 8; ++j) x[j] = trstep<ROR8>(x[j], x[j + 8], lane & 8);
#pragma unroll
  for (int j = 0; j < 4; ++j) x[j] = trstep<HMIR>(x[j], x[j + 4], lane & 4);
#pragma unroll
  for (int j = 0; j < 2; ++j) x[j] = trstep<XOR2>(x[j], x[j + 2], lane & 2);
  const float d = trstep<XOR1>(x[0], x[1], lane & 1);
  return rbf(rbf(d) * 0.08838834764831845f);
}
template <int J> __device__ __forceinline__ v4u pv_diag(u32 phl, const v4u& sel) {
  const u32 t = dppu<BCAST + J>(phl);
  return v4u{__builtin_amdgcn_perm(t, t, sel.x), __builtin_amdgcn_perm(t, t, sel.y), __builtin_amdgcn_perm(t, t, sel.z), __builtin_amdgcn_perm(t, t, sel.w)};
}
#define HOLD(T) "pace%=:\n\ts_memrealtime s[40:41]\n\ts_waitcnt lgkmcnt(0)\n\ts_sub_u32 s40, s40, " T "\n\ts_cmp_ge_i32 s40, 0\n\t" \
                "s_cbranch_scc1 paced%=\n\ts_sleep 1\n\ts_branch pace%=\npaced%=:\n\t"
__device__ __forceinline__ void hold(u32 t) { asm volatile(HOLD("%0") :: "s"(t) : "s40", "s41", "scc"); }
__device__ __forceinline__ void dma4(rsrc_t rs, const v4u& vo, u32 soff, u32 lds) {
  u32 keep;
  asm volatile(
      "s_mov_b32 %0, m0\n\ts_mov_b32 m0, %7\n\ts_nop 0\n\t"
      "buffer_load_dwordx4 %1, %5, %6 offen nt lds\n\tbuffer_load_dwordx4 %2, %5, %6 offen offset:1024 nt lds\n\t"
      "buffer_load_dwordx4 %3, %5, %6 offen offset:2048 nt lds\n\tbuffer_load_dwordx4 %4, %5, %6 offen offset:3072 nt lds\n\t"
      "s_mov_b32 m0, %0"
      : "=&s"(keep) : "v"(vo.x), "v"(vo.y), "v"(vo.z), "v"(vo.w), "s"(rs), "s"(soff), "s"(lds) : "memory");
}
#define LOAD4(A, B, C, D, RS, SO) \
  "buffer_load_dwordx4 %0, " A ", " RS ", " SO " offen nt\n\tbuffer_load_dwordx4 %1, " B ", " RS ", " SO " offen offset:1024 nt\n\t" \
  "buffer_load_dwordx4 %2, " C ", " RS ", " SO " offen offset:2048 nt\n\tbuffer_load_dwordx4 %3, " D ", " RS ", " SO " offen offset:3072 nt\n\t"
__device__ __forceinline__ void load4_first(v4u* d, rsrc_t rs, const v4u& vo, u32 soff) {
  asm volatile(LOAD4("%4", "%5", "%6", "%7", "%8", "%9")
               : "=&v"(d[0]), "=&v"(d[1]), "=&v"(d[2]), "=&v"(d[3]) : "v"(vo.x), "v"(vo.y), "v"(vo.z), "v"(vo.w), "s"(rs), "s"(soff));
}
__device__ __forceinline__ void load4(v4u* d, rsrc_t rs, const v4u& vo, u32 soff) {
  asm volatile(LOAD4("%4", "%5", "%6", "%7", "%8", "%9")
               : "+v"(d[0]), "+v"(d[1]), "+v"(d[2]), "+v"(d[3]) : "v"(vo.x), "v"(vo.y), "v"(vo.z), "v"(vo.w), "s"(rs), "s"(soff));
}
template <int N> __device__ __forceinline__ void landed(v4u* d) {
  asm volatile("s_waitcnt vmcnt(%4)" : "+v"(d[0]), "+v"(d[1]), "+v"(d[2]), "+v"(d[3]) : "i"(N));
}
template <int NL, int ND> __device__ __forceinline__ void step4(v4u* d, v4u* u, rsrc_t rs, const v4u& vo, u32 soff, u32 t, u32 live) {
  asm volatile(
      "s_cmp_eq_u32 %15, 0\n\ts_cbranch_scc1 dead%=\n" HOLD("%14") LOAD4("%8", "%9", "%10", "%11", "%12", "%13")
      "s_waitcnt vmcnt(%16)\n\ts_branch done%=\n"
      "dead%=:\n\ts_waitcnt vmcnt(%17)\n"
      "done%=:"
      : "+v"(d[0]), "+v"(d[1]), "+v"(d[2]), "+v"(d[3]), "+v"(u[0]), "+v"(u[1]), "+v"(u[2]), "+v"(u[3])
      : "v"(vo.x), "v"(vo.y), "v"(vo.z), "v"(vo.w), "s"(rs), "s"(soff), "s"(t), "s"(live), "i"(NL), "i"(ND) : "s40", "s41", "scc");
}

__global__ void __launch_bounds__(512, 2) fused_block(Params p) {
  __shared__ __attribute__((aligned(16))) Lds L_;
  LAS Lds* L = (LAS Lds*)&L_;
  const int lane = threadIdx.x & 63, wave = __builtin_amdgcn_readfirstlane(threadIdx.x >> 6);
  const int r = lane >> 4, c = lane & 15;
  const int xl = blockIdx.x & 7, l = blockIdx.x >> 3;
  const int bh = (l >> 2) * 8 + xl, b = bh >> 5, h = bh & 31, s = l & 3;
  u32 t0 = now();

  if (wave >= 4) {
    const int w = wave - 4;
    const rsrc_t rk = mkrs(p.K + (size_t)bh * (8192 * 128), 8192 * 256), rv = mkrs(p.V + (size_t)bh * (8192 * 128), 8192 * 256);
    const u32 v0 = s * 16384 + w * 1024 + lane * 16;
    const v4u vo = {v0, v0 + 3072, v0 + 2 * 3072, v0 + 3 * 3072};
    const int resume = KSTART + ((PAUSE_G * PACE) >> 4) + (l < 12 ? PAUSE_F : l >= 24 ? PAUSE_R : 0);
    auto due = [&](int g) { const int d = KSTART + ((g * PACE) >> 4); return g < PAUSE_G ? d : max(d, resume + (((g - PAUSE_G) * FAST) >> 4)); };
    v4u a[48];
    auto issue = [&](auto G) {
      constexpr int g = G;
      hold(t0 + due(g));
      if constexpr (g < 20) asm volatile("s_waitcnt vmcnt(%0)" :: "i"(g < DEPTH_G ? DEPTH - 4 : 28));
      if constexpr (g < 8) dma4(rk, vo, g * 65536, (u32)(uintptr_t)&L->kst[w][4 * g][0]);
      else if constexpr (g < 20) load4_first(a + 4 * slot_of(g), rk, vo, g * 65536);
      else load4(a + 4 * slot_of(g), g < 32 ? rk : rv, vo, (g & 31) * 65536);
      __builtin_amdgcn_sched_barrier(0);
    };
    __builtin_amdgcn_s_barrier();
    Unroll<0, 20>::run(issue);
    lds_wait(&L->cnt[C_Q], 1);
    t0 += __builtin_amdgcn_readfirstlane(L->cnt[C_SHIFT]);
    const v4u q = *(LAS v4u*)&L->qd[4 * c];
    float S[8], x[16];
    auto use = [&](auto G, auto LAST) {
      constexpr int g = G;
      if constexpr (g >= 8) landed<4 * (LAST - g)>(a + 4 * slot_of(g));
#pragma unroll
      for (int k = 0; k < 4; ++k) {
        if constexpr (g < 8) x[4 * (g & 3) + k] = qk(L->kst[w][4 * g + k][lane], q);
        else x[4 * (g & 3) + k] = qk(a[4 * slot_of(g) + k], q);
      }
      if constexpr ((g & 3) == 3) S[g >> 2] = tile_scores(x, lane);
      __builtin_amdgcn_sched_barrier(0);
    };
    Unroll<8, 12>::run([&](auto U) { use(U, std::integral_constant<int, 19>{}); });
    asm volatile("" ::: "memory");
    issue(std::integral_constant<int, 20>{});
    Unroll<0, 4>::run([&](auto U) { use(U, std::integral_constant<int, 20>{}); });
    issue(std::integral_constant<int, 21>{});
    Unroll<4, 8>::run([&](auto U) { use(U, std::integral_constant<int, 21>{}); });
    Unroll<22, 32 + LAG>::run([&](auto G) {
      issue(G);
      Unroll<used_after(G - 1) + 1, used_after(G) + 1>::run([&](auto U) { use(U, G); });
    });
    {
      float m = wave_max(fmaxf(fmaxf(fmaxf(S[0], S[1]), fmaxf(S[2], S[3])), fmaxf(fmaxf(S[4], S[5]), fmaxf(S[6], S[7]))));
      if (lane == 0) L->mw[w] = m;
      lds_arrive(&L->cnt[C_MAX]);
      lds_wait(&L->cnt[C_MAX], 4);
      const v4f m4 = *(LAS v4f*)L->mw;
      const float ml = fmaxf(fmaxf(m4.x, m4.y), fmaxf(m4.z, m4.w)) * 1.4426950408889634f;
      float ls = 0.f;
#pragma unroll
      for (int t = 0; t < 8; ++t) {
        const float pv = __builtin_amdgcn_exp2f(fmaf(S[t], 1.4426950408889634f, -ml));
        ls += pv;
        S[t] = __uint_as_float(pack2(pv, pv - rbf(pv)));
      }
      ls = wave_sum(ls);
      if (lane == 0) L->lw[w] = ls;
      lds_arrive(&L->cnt[C_SUM]);
    }
    v4f acc = {0.f, 0.f, 0.f, 0.f};
    v4u sel;
    {
      const u32 pick = (c & 8) ? 0x0302u : 0x0100u, own = (c & 1) ? (pick << 16) | 0x0c0cu : 0x0c0c0000u | pick;
#pragma unroll
      for (int d = 0; d < 4; ++d) sel[d] = ((c & 7) >> 1) == d ? own : 0x0c0c0c0cu;
    }
#pragma unroll 1
    for (int it = 0; it < 4; ++it) {
      Unroll<0, 8>::run([&](auto J) {
        constexpr int j = J, g = 32 + LAG + j;
        v4u bm[4];
        Unroll<0, 4>::run([&](auto K) { bm[K] = pv_diag<4 * (j & 3) + K>(__float_as_uint(S[j >> 2]), sel); pin(bm[K]); });
        step4<4 * LAG, 4 * (7 - j)>(a + 4 * slot_of(g), a + 4 * slot_of(32 + j), rv, vo, (LAG + j + 8 * it) * 65536, t0 + due(g + 8 * it), j + LAG < 8 ? 1 : 3 - it);
#pragma unroll
        for (int k = 0; k < 4; ++k)
          acc = __builtin_amdgcn_mfma_f32_16x16x32_bf16(__builtin_bit_cast(bf16x8, a[4 * slot_of(32 + j) + k]), __builtin_bit_cast(bf16x8, bm[k]), acc, 0, 0, 0);
        pin(acc);
        __builtin_amdgcn_sched_barrier(0);
      });
#pragma unroll
      for (int t = 0; t < 6; ++t) S[t] = S[t + 2];
    }
    LAS float* o = &L->op[w][32 * r + 2 * (c & 8) + (c & 7)];
    const float o0 = acc.x + dppf<ROR8>(acc.x), o1 = acc.y + dppf<ROR8>(acc.y), o2 = acc.z + dppf<ROR8>(acc.z), o3 = acc.w + dppf<ROR8>(acc.w);
    o[0] = (c & 8) ? o2 : o0; o[8] = (c & 8) ? o3 : o1;
    wg_barrier();
    return;
  }

  const int w = wave, t = w * 64 + lane;
  const rsrc_t rs = mkrs(p.ws, WS_END), rm = mkrs(p.wmod, 512 * 3072 * 2);
  const bool first = l >= 16 && w >= 2;
  v2u c0 = {0u, 0u}, c1 = c0;
  v4u W[32];
  if (first) {
    const int row = 64 * xl + 32 * (w - 2) + 4 * (lane >> 3);
    c0 = ((const GAS v2u*)p.c)[row >> 2]; c1 = ((const GAS v2u*)p.c)[128 + (row >> 2)];
#pragma unroll
    for (int k = 0; k < 4; ++k) W[k] = ldw(rm, row * 6144 + (lane & 7) * 16, k * 6144 + (l - 16) * 128);
  }
  const u32 epoch = __hip_atomic_load((GAS u32*)(p.ws + WS_EPOCH), __ATOMIC_RELAXED, __HIP_MEMORY_SCOPE_AGENT);
  const int j8 = 2 * s + (w & 1);
  v4u xs = {0u, 0u, 0u, 0u};
  u32 xo = 0;
  if (w < 2) {
    xs = ((const GAS v4u*)p.x)[b * 64 + lane];
    xo = ((const GAS u32*)p.x)[b * 256 + 32 * j8 + 8 * r + (c & 7)];
    const rsrc_t rq = mkrs(p.wq, 512 * 4096 * 2);
#pragma unroll
    for (int k = 0; k < 16; ++k) W[k] = ldw(rq, (64 * j8 + 16 * r) * 8192 + h * 256 + c * 16, k * 8192);
  }
  __builtin_amdgcn_sched_barrier(0);
  if (threadIdx.x < 16) L->cnt[threadIdx.x] = 0;
  wg_barrier();
  __builtin_amdgcn_sched_barrier(0);
  if (w == 3 && l == 12) {
    u64 pc;
    asm volatile("s_getpc_b64 %0" : "=s"(pc));
#pragma unroll
    for (int k = 0; k < CODE_KB; ++k) W[k] = ((const GAS v4u*)(pc & ~127ull))[k * 64 + lane];
#pragma unroll
    for (int k = 0; k < CODE_KB; ++k) pin(W[k]);
  }
  const u32 tag = ((u32)__builtin_amdgcn_readfirstlane(epoch) | 1u) + 2u;
  if (l == 0 && w == 3) publish(rs, lane == 0 ? WS_T + xl * 16 : OOB, t0, t0, tag);
  if (first) {
    float v[16] = {0.f, 0.f, 0.f, 0.f, 0.f, 0.f, 0.f, 0.f, 0.f, 0.f, 0.f, 0.f, 0.f, 0.f, 0.f, 0.f};
    rows2(W[0], W[1], silu2(c0.x), silu2(c1.x), v);
    rows2(W[2], W[3], silu2(c0.y), silu2(c1.y), v);
#pragma unroll
    for (int j = 0; j < 8; ++j) v[j] = sum32(v[j], v[j + 8]);
#pragma unroll
    for (int j = 0; j < 4; ++j) v[j] = sum16(v[j], v[j + 4]);
    L->red1[w][lane] = v2f{trstep<ROR8>(v[0], v[2], lane & 8), trstep<ROR8>(v[1], v[3], lane & 8)};
    if (last_of(&L->cnt[C_MP], 2, lane)) {
      const int src = (lane & 32) + (lane & 3) * 8 + ((lane >> 2) & 7);
      publish(rs, WS_MP + ((l - 16) * 8 + xl) * 1024 + lane * 16, L->red1[2][src] + L->red1[3][src], tag);
    }
  }
  if (w < 2) {
    v2f mean, rstd;
    {
      v2f e[8];
#pragma unroll
      for (int k = 0; k < 4; ++k) { e[2 * k] = v2f{bflo(xs[k]), bflo(xs[k])}; e[2 * k + 1] = v2f{bfhi(xs[k]), bfhi(xs[k])}; }
      ln_stats(e, lane, mean, rstd);
      pin(mean); pin(rstd);
    }
#pragma unroll
    for (int k = 0; k < 8; ++k) { interleave(W[2 * k], W[2 * k + 1]); pin(W[2 * k]); pin(W[2 * k + 1]); }
    u32 off[8];
#pragma unroll
    for (int k = 0; k < 8; ++k) off[k] = WS_MP + (((c & 8) + j8) * 8 + k) * 1024 + b * 512 + (8 * r + (c & 7)) * 16;
    v4u val[8];
    hold(t0 + MOD_T);
    gather<8, 2>(rs, off, val, tag, t0);
    v2f m = payload(val[0]);
#pragma unroll
    for (int k = 1; k < 8; ++k) m += payload(val[k]);
    const u32 own = pack2(m.x, m.y), oth = dppu<ROR8>(own);
    const v2f hv = modulate(unpack2(xo), mean, rstd, rbf2(unpack2((c & 8) ? own : oth) + 1.f), unpack2((c & 8) ? oth : own));
    float u[4];
    gemv<8>(W, pack2(hv.x, hv.y), u);
    L->red1[w][lane] = v2f{sum16(u[0], u[2]), sum16(u[1], u[3])};
    if (last_of(&L->cnt[C_QP], 2, lane)) {
      publish(rs, WS_QP + (bh * 4 + s) * 1024 + lane * 16, L->red1[0][lane] + L->red1[1][lane], tag);
      lds_arrive(&L->cnt[C_QPUB]);
    }
  } else if (w == 2) {
    const u32 o0 = WS_QP + bh * 4096 + lane * 16, off[5] = {o0, o0 + 1024, o0 + 2048, o0 + 3072, WS_T + (lane & 7) * 16};
    v4u val[5];
    lds_wait(&L->cnt[C_QPUB], 1);
    __builtin_amdgcn_s_sleep(D_Q);
    gather<5, 2>(rs, off, val, tag, t0);
    int d = (int)(val[4].x - t0);
    d = min(d, (int)dppu<XOR1>(d)); d = min(d, (int)dppu<XOR2>(d)); d = min(d, (int)dppu<HMIR>(d));
    L->cnt[C_SHIFT] = max(min(d, 0), -200);
    const v2f qv = (payload(val[0]) + payload(val[1])) + (payload(val[2]) + payload(val[3]));
    L->qd[4 * c + r] = pack2(qv.x, qv.y);
    lds_arrive(&L->cnt[C_Q]);
  }

  const int fstart = (43 * xl + 3) >> 2, fg = fstart + l;
  const bool ffn = fg < (43 * (xl + 1) + 3) >> 2;
  v4u F[8];
  v4u W2[4];
#pragma unroll
  for (int k = 0; k < 8; ++k) F[k] = v4u{0u, 0u, 0u, 0u};
#pragma unroll
  for (int k = 0; k < 4; ++k) W2[k] = v4u{0u, 0u, 0u, 0u};
  v4u Wo[8];
  lds_wait(&L->cnt[C_Q], 1);
  hold(t0 + LATE_T);
#pragma unroll
  for (int k = 0; k < 8; ++k) Wo[k] = ldw(mkrs(p.wo, 4096 * 512 * 2), (h * 128 + 32 * s + 2 * r) * 1024 + w * 256 + c * 16, (k / 2) * 8192 + (k & 1) * 1024);
  __builtin_amdgcn_sched_barrier(0);
  if (l >= 24) {
    const int rg = xl * 8 + l - 24, row = 128 * w + 8 * (lane >> 2);
    const v4u ca = ((const GAS v4u*)p.c)[row >> 3], cb = ((const GAS v4u*)p.c)[64 + (row >> 3)];
#pragma unroll
    for (int k = 0; k < 8; ++k) W[k] = ldw(rm, row * 6144 + (lane & 3) * 16, k * 6144 + 2048 + rg * 64);
    float v[16] = {0.f, 0.f, 0.f, 0.f, 0.f, 0.f, 0.f, 0.f, 0.f, 0.f, 0.f, 0.f, 0.f, 0.f, 0.f, 0.f};
#pragma unroll
    for (int k = 0; k < 4; ++k) rows2(W[2 * k], W[2 * k + 1], silu2(ca[k]), silu2(cb[k]), v);
#pragma unroll
    for (int j = 0; j < 8; ++j) v[j] = sum32(v[j], v[j + 8]);
#pragma unroll
    for (int j = 0; j < 4; ++j) { v[j] = sum16(v[j], v[j + 4]); v[j] += dppf<ROR8>(v[j]); v[j] += dppf<ROR4>(v[j]); }
    if ((lane & 12) == 0) L->red1b[w][4 * r + (lane & 3)] = v4f{v[0], v[1], v[2], v[3]};
    if (last_of(&L->cnt[C_MOD], 4, lane)) {
      const int src = (c & 8) + (c & 1) * 4 + ((c >> 1) & 3);
      const v4f a = (L->red1b[0][src] + L->red1b[1][src]) + (L->red1b[2][src] + L->red1b[3][src]);
      publish(rs, lane < 16 ? WS_MOD + (c >> 3) * 8192 + (8 * rg + (c & 7)) * 16 : OOB, pack2(a.x, a.y), pack2(a.z, a.w), tag);
    }
  } else if (ffn) {
    const rsrc_t r1 = mkrs(p.w1, 512 * 1376 * 2), r3 = mkrs(p.w3, 512 * 1376 * 2), r2 = mkrs(p.w2, 1376 * 512 * 2);
#pragma unroll
    for (int k = 0; k < 8; ++k) F[k] = ldw(k < 4 ? r1 : r3, (128 * w + (lane >> 1)) * 2752 + (lane & 1) * 16, (k & 3) * 32 * 2752 + fg * 32);
#pragma unroll
    for (int k = 0; k < 4; ++k) W2[k] = ldw(r2, r * 4096 + w * 256 + c * 16, (16 * fg + k) * 1024);
    LAS v4s* tr = (LAS v4s*)&L->stage[w][0] + (8 * r + (c >> 2)) * 4 + (c & 3);
#pragma unroll
    for (int k = 0; k < 8; ++k) {
      L->stage[w][lane] = F[k];
      asm volatile("" ::: "memory");
      const v2u lo = __builtin_bit_cast(v2u, __builtin_amdgcn_ds_read_tr16_b64_v4i16(tr));
      const v2u hi = __builtin_bit_cast(v2u, __builtin_amdgcn_ds_read_tr16_b64_v4i16(tr + 16));
      F[k] = v4u{lo.x, lo.y, hi.x, hi.y};
      asm volatile("" : "+v"(F[k]) :: "memory");
    }
    interleave(W2[0], W2[1]); interleave(W2[2], W2[3]);
  }
#pragma unroll
  for (int k = 0; k < 4; ++k) { interleave(Wo[2 * k], Wo[2 * k + 1]); pin(Wo[2 * k]); pin(Wo[2 * k + 1]); }

  const bool red = l < 4;
  const int j = xl * 4 + (l & 3), rp = 64 * (j >> 3) + 16 * (j & 3) + 4 * (lane & 3) + 2 * ((j >> 2) & 1) + ((lane >> 2) & 1);
  const int tp = w * 64 + 16 * ((lane >> 3) & 3) + 4 * (lane & 3) + 2 * (lane >> 5) + ((lane >> 2) & 1);
  v2f sc0 = {0.f, 0.f}, sc1 = sc0, sh0 = sc0, sh1 = sc0;
  float xr = 0.f, gar = 0.f, gbr = 0.f;
  if (ffn) {
    const u32 modb = WS_MOD + (1 + (lane >> 5)) * 2048 + (32 * w + (lane & 31)) * 16;
    const u32 base = WS_MOD + (r >> 1) * 8192 + (rp >> 1) * 16;
    const u32 off[4] = {modb, modb + 8192, (red && w == 0) ? base : OOB, (red && w == 0) ? base + 3 * 2048 : OOB};
    v4u val[4];
    hold(t0 + MOD2_T);
    const u32 xv = ((const GAS u32*)p.x)[(r >> 1) * 256 + rp];
    gather<4, 16, 2>(rs, off, val, tag, t0);
    auto fetch = [&](int from, const v4u& u) {
      u32 lo = __builtin_amdgcn_ds_bpermute(from, u.x), hi = __builtin_amdgcn_ds_bpermute(from, u.z);
      pin(lo); pin(hi);
      return (tp & 1) ? hi : lo;
    };
    const int src = (tp & 62) * 2;
    const u32 h0 = fetch(src, val[0]), h1 = fetch(src, val[1]), g0 = fetch(src + 128, val[0]), g1 = fetch(src + 128, val[1]);
    sc0 = rbf2(v2f{bflo(g0), bflo(g1)} + 1.f); sc1 = rbf2(v2f{bfhi(g0), bfhi(g1)} + 1.f);
    sh0 = v2f{bflo(h0), bflo(h1)}; sh1 = v2f{bfhi(h0), bfhi(h1)};
    const u32 ga = (rp & 1) ? val[2].z : val[2].x, gb = (rp & 1) ? val[3].z : val[3].x;
    xr = (r & 1) ? bfhi(xv) : bflo(xv); gar = (r & 1) ? bfhi(ga) : bflo(ga); gbr = (r & 1) ? bfhi(gb) : bflo(gb);
  }
  float scale = 0.f;
  if (w == 0) {
    lds_wait(&L->cnt[C_SUM], 4);
    const v4f m4 = *(LAS v4f*)L->mw, l4 = *(LAS v4f*)L->lw;
    const float m = fmaxf(fmaxf(m4.x, m4.y), fmaxf(m4.z, m4.w));
    publish(rs, lane == 0 ? WS_ML + (bh * 4 + s) * 16 : OOB, v2f{m, (l4.x + l4.y) + (l4.z + l4.w)}, tag);
    const u32 off[1] = {WS_ML + (bh * 4 + (lane & 3)) * 16};
    v4u val[1];
    __builtin_amdgcn_s_sleep(D_ML);
    gather<1, 16>(rs, off, val, tag, t0);
    const v2f ml = payload(val[0]);
    float M = fmaxf(ml.x, dppf<XOR1>(ml.x)); M = fmaxf(M, dppf<XOR2>(M));
    float Ls = ml.y * __builtin_amdgcn_exp2f((ml.x - M) * 1.4426950408889634f);
    Ls += dppf<XOR1>(Ls); Ls += dppf<XOR2>(Ls);
    scale = __builtin_amdgcn_exp2f((m - M) * 1.4426950408889634f) / Ls;
  }

  u32 o_h = WS_O + (bh * 4 + s) * 1024 + lane * 16, o_hg[1] = {WS_O + (bh * 4 + (c >> 2)) * 1024 + (16 * s + 4 * r + (c & 3)) * 16};
  u32 o_y = (r & 1) ? OOB : WS_Y + ((8 * w + 2 * r + (c >> 2)) * 256 + bh * 4 + s) * 64 + (c & 3) * 16;
  u32 o_yg = WS_Y + j * 16384 + w * 4096 + lane * 16;
  u32 o_x = (lane >> 3) < REP_X1 ? WS_X1 + (lane >> 3) * 4096 + j * 128 + (lane & 7) * 16 : OOB;
  u32 o_xg[4];
#pragma unroll
  for (int k = 0; k < 4; ++k) { o_xg[k] = WS_X1 + (xl & (REP_X1 - 1)) * 4096 + lane * 16 + ((w + k) & 3) * 1024; pin(o_xg[k]); }
  u32 o_o = WS_OP + ((8 * w + 4 * (r >> 1) + (c >> 2)) * 96 + fg) * 128 + (r & 1) * 64 + (c & 3) * 16;
  u32 o_og[3];
  o_og[0] = WS_OP + j * 12288 + w * 3072 + lane * 16;
  o_og[1] = (w < 3 || (lane >> 3) < NFFN - 80) ? o_og[0] + 1024 : OOB;
  o_og[2] = w < 3 ? o_og[0] + 2048 : OOB;
  u32 o_e = (j == 0 && lane == 0) ? WS_EPOCH : OOB;
  u32 o_out = (lane & 8) == 0 ? ((r >> 1) * 512 + 2 * rp + (r & 1)) * 2 : OOB;
  const rsrc_t rout = mkrs(p.out, 2048);
  LAS v2f* a_o = (LAS v2f*)&L->op[0][32 * r + 8 * (c & 3) + 2 * (c >> 2)];
  LAS float* a_rw = &L->ry[w][4 * r + (lane & 3)];
  LAS float* a_rr = &L->ry[r & 2][2 * (lane & 4) + 4 * (r & 1) + (lane & 3)];
  LAS u32* a_hw = &L->h2[0][tp];
  LAS v4u* a_hr = (LAS v4u*)&L->h2[c & 1][64 * w + 4 * r];
  LAS float* a_gw = &L->red2[lane][w];
  LAS v4f* a_gr = (LAS v4f*)L->red2[(c & 4) * 4 + 4 * r + (c & 3)];
  LAS float* a_ow = &L->ro[w][8 * r + (lane & 7)];
  LAS float* a_or = &L->ro[0][8 * r + (lane & 7)];
  pin(o_h); pin(o_hg[0]); pin(o_y); pin(o_yg); pin(o_x); pin(o_o); pin(o_og[0]); pin(o_og[1]); pin(o_og[2]); pin(o_e); pin(o_out);
  pin(scale); pin(sc0); pin(sc1); pin(sh0); pin(sh1); pin(xr); pin(gar); pin(gbr);
  pin(a_o); pin(a_rw); pin(a_rr); pin(a_hw); pin(a_hr); pin(a_gw); pin(a_gr); pin(a_ow); pin(a_or);

  wg_barrier();
  {
    if (w == 0) publish(rs, o_h, ((a_o[0] + a_o[64]) + (a_o[128] + a_o[192])) * scale, tag);
    v4u val[1];
    __builtin_amdgcn_s_sleep(D_H);
    gather<1, 0>(rs, o_hg, val, tag, t0);
    float o0 = __uint_as_float(val[0].x), o1 = __uint_as_float(val[0].z);
    o0 += dppf<ROR4>(o0); o1 += dppf<ROR4>(o1); o0 += dppf<ROR8>(o0); o1 += dppf<ROR8>(o1);
    float u[4];
    gemv<4>(Wo, pack2(o0, o1), u);
    publish(rs, o_y, pack2(sum16(u[0], u[0]), sum16(u[1], u[1])), pack2(sum16(u[2], u[2]), sum16(u[3], u[3])), tag);
  }
  if (ffn) {
    float x1r = 0.f;
    {
      const u32 off[4] = {o_yg, o_yg + 1024, o_yg + 2048, o_yg + 3072};
      v4u val[4];
      __builtin_amdgcn_s_sleep(D_Y);
      gather<4, 0>(rs, off, val, tag, t0);
      const v2f lo = (unpack2(val[0].x) + unpack2(val[1].x)) + (unpack2(val[2].x) + unpack2(val[3].x));
      const v2f hi = (unpack2(val[0].z) + unpack2(val[1].z)) + (unpack2(val[2].z) + unpack2(val[3].z));
      float a = sum16(sum32(lo.x, hi.x), sum32(lo.y, hi.y));
      a += dppf<ROR8>(a); a += dppf<ROR4>(a);
      a_rw[0] = a;
      wg_barrier();
      if (red && w == 0) {
        x1r = rbf(xr + rbf(gar * rbf(a_rr[0] + a_rr[16])));
        const auto e = __builtin_amdgcn_permlane16_swap(__float_as_uint(x1r), __float_as_uint(x1r), false, false);
        const u32 pr = (e[0] >> 16) | e[1];
        const auto bb = __builtin_amdgcn_permlane32_swap(pr, pr, false, false);
        publish(rs, o_x, bb[0], bb[1], tag);
        __builtin_amdgcn_raw_buffer_store_b32(tag, rs, o_e, 0, AUX_SC1);
        __builtin_amdgcn_s_sleep(D_XR);
      } else __builtin_amdgcn_s_sleep(D_X);
    }
    v4u val[4];
    gather<4, 0>(rs, o_xg, val, tag, t0);
    v2f e[8], mean, rstd;
#pragma unroll
    for (int k = 0; k < 4; ++k) { e[2 * k] = v2f{bflo(val[k].x), bflo(val[k].z)}; e[2 * k + 1] = v2f{bfhi(val[k].x), bfhi(val[k].z)}; }
    ln_stats(e, lane, mean, rstd);
    const v2f ha = modulate(e[0], mean, rstd, sc0, sh0), hb = modulate(e[1], mean, rstd, sc1, sh1);
    a_hw[0] = pack2(ha.x, hb.x); a_hw[256] = pack2(ha.y, hb.y);
    asm volatile("" ::: "memory");
    v4u A[4];
#pragma unroll
    for (int k = 0; k < 4; ++k) A[k] = a_hr[4 * k];
    __builtin_amdgcn_sched_barrier(0);
    v4f dg = {0.f, 0.f, 0.f, 0.f}, du = dg;
#pragma unroll
    for (int k = 0; k < 4; ++k) {
      dg = __builtin_amdgcn_mfma_f32_16x16x32_bf16(__builtin_bit_cast(bf16x8, A[k]), __builtin_bit_cast(bf16x8, F[k]), dg, 0, 0, 0);
      du = __builtin_amdgcn_mfma_f32_16x16x32_bf16(__builtin_bit_cast(bf16x8, A[k]), __builtin_bit_cast(bf16x8, F[4 + k]), du, 0, 0, 0);
    }
    const float lo = (lane & 16) ? dg.y : dg.x, hi = (lane & 16) ? du.y : du.x;
    a_gw[0] = (lane & 32) ? hi : lo;
    wg_barrier();
    const v4f g4 = a_gr[0], u4 = a_gr[32];
    const v2f gu = rbf2(v2f{(g4.x + g4.y) + (g4.z + g4.w), (u4.x + u4.y) + (u4.z + u4.w)});
    const float hh = rbf(silu(gu.x)) * gu.y;
    u32 hp = pack2(hh, dppf<XOR1>(hh));
    float o[16] = {0.f, 0.f, 0.f, 0.f, 0.f, 0.f, 0.f, 0.f, 0.f, 0.f, 0.f, 0.f, 0.f, 0.f, 0.f, 0.f};
    dpp_ready(hp);
    dot8<0>(W2[0], W2[1], hp, o); dot8<4>(W2[0], W2[1], hp, o + 8); dot8<2>(W2[2], W2[3], hp, o); dot8<6>(W2[2], W2[3], hp, o + 8);
    dots_done(o); dots_done(o + 8);
#pragma unroll
    for (int k = 0; k < 4; ++k) { o[k] = sum32(o[k], o[k + 4]); o[8 + k] = sum32(o[8 + k], o[12 + k]); }
    publish(rs, o_o, pack2(sum16(o[0], o[2]), sum16(o[1], o[3])), pack2(sum16(o[8], o[10]), sum16(o[9], o[11])), tag);
    if (red) {
      v4u va[3];
      __builtin_amdgcn_s_sleep(D_O);
      gather<3, 0, 1>(rs, o_og, va, tag, t0);
      const v2f b0 = unpack2(va[0].x) + unpack2(va[1].x) + unpack2(va[2].x), b1 = unpack2(va[0].z) + unpack2(va[1].z) + unpack2(va[2].z);
      float a = sum16(sum32(b0.x, b1.x), sum32(b0.y, b1.y));
      a += dppf<ROR8>(a);
      a_ow[0] = a;
      wg_barrier();
      if (w == 0) {
        const float res = x1r + rbf(gbr * rbf((a_or[0] + a_or[32]) + (a_or[64] + a_or[96])));
        __builtin_amdgcn_raw_buffer_store_b16((u16)pack2(res, res), rout, o_out, 0, 0);
      }
    }
  }
}

extern "C" void kernel_launch(void* const* d_in, const int* in_sizes, int n_in, void* d_out, int out_size, void* d_ws, size_t ws_size, hipStream_t stream) {
  Params p;
  p.x = (const u16*)d_in[0]; p.K = (const u16*)d_in[1]; p.V = (const u16*)d_in[2]; p.wq = (const u16*)d_in[3]; p.wo = (const u16*)d_in[4];
  p.w1 = (const u16*)d_in[6]; p.w3 = (const u16*)d_in[7]; p.w2 = (const u16*)d_in[8]; p.c = (const u16*)d_in[9]; p.wmod = (const u16*)d_in[10];
  p.out = (u16*)d_out; p.ws = (unsigned char*)d_ws;
  hipLaunchKernelGGL(fused_block, dim3(256), dim3(512), 0, stream, p);
}
```

```cpp
#include <hip/hip_runtime.h>
#include <stdint.h>
#include <type_traits>

typedef unsigned short u16;
typedef unsigned int u32;
typedef unsigned long long u64;
typedef u32 v4u __attribute__((ext_vector_type(4)));
typedef u32 v2u __attribute__((ext_vector_type(2)));
typedef float v4f __attribute__((ext_vector_type(4)));
typedef float v2f __attribute__((ext_vector_type(2)));
typedef __bf16 bf16x2 __attribute__((ext_vector_type(2)));
typedef __bf16 bf16x8 __attribute__((ext_vector_type(8)));
typedef short v4s __attribute__((ext_vector_type(4)));
typedef __amdgpu_buffer_rsrc_t rsrc_t;
#define GAS __attribute__((address_space(1)))
#define LAS __attribute__((address_space(3)))

constexpr int KSTART = 60;
constexpr int PACE = 945;
constexpr int DEPTH = 8, DEPTH_G = 11;
constexpr int LAG = 7;
constexpr int PAUSE_G = 15, PAUSE_F = 320, PAUSE_R = 150, FAST = 600;
constexpr int MOD_T = 450;
constexpr int STRIP_T = 960, WO_T = 1960, WO_DT = 65, MOD_K = 1, ML_K = 5;
constexpr int D_Q = 30, D_Y = 15, D_X = 19, D_XR = 16, D_O = 16;
constexpr u32 SPIN_TICKS = 400000;
constexpr int CODE_KB = 29;
constexpr int due_of(int g) { return KSTART + ((g * PACE) >> 4); }
constexpr int slot_of(int g) { return g < 20 ? g - 8 : (g - 20) & 7; }
constexpr int used_after(int g) { return (g <= 21 || g - LAG < 11) ? 11 : g - LAG; }

constexpr int NFFN = 86, REP_X1 = 4;
constexpr u32 WS_EPOCH = 0;
constexpr u32 WS_MP = 256;
constexpr u32 WS_MOD = WS_MP + 16 * 8 * 1024;
constexpr u32 WS_ML = WS_MOD + 2 * 8192;
constexpr u32 WS_T = WS_ML + 4096;
constexpr u32 WS_X1 = WS_T + 128;
constexpr u32 WS_QP = WS_X1 + REP_X1 * 4096;
constexpr u32 WS_Y = WS_QP + 256 * 1024;
constexpr u32 WS_OP = WS_Y + 32 * 256 * 64;
constexpr u32 WS_END = WS_OP + 32 * 96 * 128;
constexpr u32 OOB = 0x7ffffff0u;
constexpr int AUX_SC1 = 16, AUX_NT = 2;

struct Params {
  const u16 *x, *K, *V, *wq, *wo, *w1, *w3, *w2, *c, *wmod;
  u16* out;
  unsigned char* ws;
};

enum { C_MP, C_Q, C_MAX, C_SUM, C_MOD, C_QP, C_QPUB, C_SHIFT };
struct Lds {
  u32 cnt[16];
  v2f red1[4][64];
  v4f red1b[4][16];
  u32 qd[64];
  float mw[4], lw[4];
  float op[4][128];
  float red2[64][4];
  u32 h2[2][256];
  float ry[4][16], ro[4][32];
  v4u stage[4][64];
  v4u kst[4][32][64];
};

__device__ __forceinline__ float bflo(u32 v) { return __uint_as_float(v << 16); }
__device__ __forceinline__ float bfhi(u32 v) { return __uint_as_float(v & 0xffff0000u); }
__device__ __forceinline__ v2f unpack2(u32 v) { return v2f{bflo(v), bfhi(v)}; }
__device__ __forceinline__ u32 pack2(float a, float b) { bf16x2 r = {(__bf16)a, (__bf16)b}; return __builtin_bit_cast(u32, r); }
__device__ __forceinline__ float rbf(float a) { return bflo(pack2(a, a)); }
__device__ __forceinline__ v2f rbf2(v2f v) { u32 r = pack2(v.x, v.y); asm("" : "+v"(r)); return unpack2(r); }
__device__ __forceinline__ float dot2(u32 a, u32 b, float acc) {
  return __builtin_amdgcn_fdot2_f32_bf16(__builtin_bit_cast(bf16x2, a), __builtin_bit_cast(bf16x2, b), acc, false);
}
__device__ __forceinline__ float silu(float v) { return v * __builtin_amdgcn_rcpf(1.f + __expf(-v)); }
__device__ __forceinline__ u32 silu2(u32 v) { return pack2(silu(bflo(v)), silu(bfhi(v))); }
template <int CTRL> __device__ __forceinline__ u32 dppu(u32 x) { return (u32)__builtin_amdgcn_mov_dpp((int)x, CTRL, 0xf, 0xf, true); }
template <int CTRL> __device__ __forceinline__ float dppf(float x) { return __uint_as_float(dppu<CTRL>(__float_as_uint(x))); }
constexpr int XOR1 = 0xB1, XOR2 = 0x4E, HMIR = 0x141, ROR4 = 0x124, ROR8 = 0x128, BCAST = 0x150;
__device__ __forceinline__ float sum32(float a, float b) {
  auto s = __builtin_amdgcn_permlane32_swap(__float_as_uint(a), __float_as_uint(b), false, false);
  return __uint_as_float(s[0]) + __uint_as_float(s[1]);
}
__device__ __forceinline__ float sum16(float a, float b) {
  auto s = __builtin_amdgcn_permlane16_swap(__float_as_uint(a), __float_as_uint(b), false, false);
  return __uint_as_float(s[0]) + __uint_as_float(s[1]);
}
template <int CTRL> __device__ __forceinline__ float trstep(float lo, float hi, bool bit) {
  const float a = lo + dppf<CTRL>(lo), b = hi + dppf<CTRL>(hi);
  return bit ? b : a;
}
__device__ __forceinline__ float wave_sum(float x) {
  x = sum32(x, x); x = sum16(x, x);
  x += dppf<ROR8>(x); x += dppf<HMIR>(x); x += dppf<XOR2>(x); x += dppf<XOR1>(x);
  return x;
}
__device__ __forceinline__ float wave_max(float x) {
  x = fmaxf(x, dppf<XOR1>(x)); x = fmaxf(x, dppf<XOR2>(x)); x = fmaxf(x, dppf<HMIR>(x)); x = fmaxf(x, dppf<ROR8>(x));
  auto s = __builtin_amdgcn_permlane16_swap(__float_as_uint(x), __float_as_uint(x), false, false);
  x = fmaxf(__uint_as_float(s[0]), __uint_as_float(s[1]));
  auto t = __builtin_amdgcn_permlane32_swap(__float_as_uint(x), __float_as_uint(x), false, false);
  return fmaxf(__uint_as_float(t[0]), __uint_as_float(t[1]));
}
__device__ __forceinline__ rsrc_t mkrs(const void* p, u32 bytes) { return __builtin_amdgcn_make_buffer_rsrc((void*)p, 0, bytes, 0x00020000); }
__device__ __forceinline__ v4u ldw(rsrc_t rs, u32 voff, u32 soff) { return __builtin_amdgcn_raw_buffer_load_b128(rs, voff, soff, AUX_NT); }
__device__ __forceinline__ u32 now() { return (u32)__builtin_amdgcn_s_memrealtime(); }
template <class T> __device__ __forceinline__ void pin(T& v) { asm volatile("" : "+v"(v)); }
template <int I, int E> struct Unroll {
  template <class F> static __device__ __forceinline__ void run(F&& f) {
    if constexpr (I < E) { f(std::integral_constant<int, I>{}); Unroll<I + 1, E>::run(f); }
  }
};

__device__ __forceinline__ void lds_arrive(LAS u32* c) {
  asm volatile("s_mov_b64 exec, 1\n\tds_add_u32 %0, %1\n\ts_mov_b64 exec, -1" :: "v"((u32)(uintptr_t)c), "v"(1u) : "memory");
}
__device__ __forceinline__ void lds_wait(LAS u32* c, u32 n) {
  while ((u32)__builtin_amdgcn_readfirstlane(__hip_atomic_load(c, __ATOMIC_RELAXED, __HIP_MEMORY_SCOPE_WORKGROUP)) < n) __builtin_amdgcn_s_sleep(1);
  asm volatile("" ::: "memory");
}
__device__ __forceinline__ bool last_of(LAS u32* c, u32 n, int lane) {
  asm volatile("" ::: "memory");
  u32 old = 0;
  if (lane == 0) old = __hip_atomic_fetch_add(c, 1u, __ATOMIC_RELAXED, __HIP_MEMORY_SCOPE_WORKGROUP);
  const bool last = (u32)__builtin_amdgcn_readfirstlane(old) == n - 1u;
  asm volatile("" ::: "memory");
  return last;
}
__device__ __forceinline__ void wg_barrier() { asm volatile("s_waitcnt lgkmcnt(0)\n\ts_barrier" ::: "memory"); }

__device__ __forceinline__ void publish(rsrc_t rs, u32 off, u32 a, u32 b, u32 tag) {
  __builtin_amdgcn_raw_buffer_store_b128(v4u{a, tag, b, tag}, rs, off, 0, AUX_SC1);
}
__device__ __forceinline__ void publish(rsrc_t rs, u32 off, v2f v, u32 tag) { publish(rs, off, __float_as_uint(v.x), __float_as_uint(v.y), tag); }
template <int N, int SLEEP, int NFULL = N> __device__ __forceinline__ void gather(rsrc_t rs, const u32 (&off)[N], v4u (&val)[N], u32 tag, u32 t0) {
  for (;;) {
#pragma unroll
    for (int k = 0; k < N; ++k) val[k] = __builtin_amdgcn_raw_buffer_load_b128(rs, off[k], 0, AUX_SC1);
    u32 bad = 0u;
#pragma unroll
    for (int k = 0; k < N; ++k) {
      const u32 m = (val[k].y ^ tag) | (val[k].w ^ tag);
      bad |= (k >= NFULL && off[k] == OOB) ? 0u : m;
    }
    asm("" : "+v"(bad));
    if (__builtin_expect(!__any(bad != 0u), 1) || now() - t0 > SPIN_TICKS) break;
    if (SLEEP) __builtin_amdgcn_s_sleep(SLEEP);
  }
}
__device__ __forceinline__ v2f payload(const v4u& u) { return v2f{__uint_as_float(u.x), __uint_as_float(u.z)}; }

__device__ __forceinline__ void interleave(v4u& a, v4u& b) {
  const v4u lo = {__builtin_amdgcn_perm(b.x, a.x, 0x05040100u), __builtin_amdgcn_perm(b.x, a.x, 0x07060302u),
                  __builtin_amdgcn_perm(b.y, a.y, 0x05040100u), __builtin_amdgcn_perm(b.y, a.y, 0x07060302u)};
  const v4u hi = {__builtin_amdgcn_perm(b.z, a.z, 0x05040100u), __builtin_amdgcn_perm(b.z, a.z, 0x07060302u),
                  __builtin_amdgcn_perm(b.w, a.w, 0x05040100u), __builtin_amdgcn_perm(b.w, a.w, 0x07060302u)};
  a = lo; b = hi;
}
__device__ __forceinline__ void rows2(v4u a, v4u b, u32 s0, u32 s1, float (&v)[16]) {
  interleave(a, b);
#pragma unroll
  for (int j = 0; j < 4; ++j) {
    v[j] = dot2(a[j], s0, v[j]); v[4 + j] = dot2(b[j], s0, v[4 + j]);
    v[8 + j] = dot2(a[j], s1, v[8 + j]); v[12 + j] = dot2(b[j], s1, v[12 + j]);
  }
}
template <int LANE> __device__ __forceinline__ void dot8(const v4u& wa, const v4u& wb, u32 vp, float* a) {
#define DOT(i, j) "v_dot2c_f32_bf16_dpp %" #i ", %8, %" #j " row_newbcast:%c17 row_mask:0xf bank_mask:0xf\n\t"
  asm(DOT(0, 9) DOT(1, 10) DOT(2, 11) DOT(3, 12) DOT(4, 13) DOT(5, 14) DOT(6, 15) DOT(7, 16)
      : "+v"(a[0]), "+v"(a[1]), "+v"(a[2]), "+v"(a[3]), "+v"(a[4]), "+v"(a[5]), "+v"(a[6]), "+v"(a[7])
      : "v"(vp), "v"(wa.x), "v"(wa.y), "v"(wa.z), "v"(wa.w), "v"(wb.x), "v"(wb.y), "v"(wb.z), "v"(wb.w), "i"(LANE));
#undef DOT
}
__device__ __forceinline__ void dpp_ready(u32& vp) { asm("s_nop 1" : "+v"(vp)); }
__device__ __forceinline__ void dots_done(float* a) {
  asm("s_nop 2" : "+v"(a[0]), "+v"(a[1]), "+v"(a[2]), "+v"(a[3]), "+v"(a[4]), "+v"(a[5]), "+v"(a[6]), "+v"(a[7]));
}
template <int NK> __device__ __forceinline__ void gemv(const v4u (&W)[32], u32 vp, float (&u)[4]) {
  float a[8] = {0.f, 0.f, 0.f, 0.f, 0.f, 0.f, 0.f, 0.f};
  dpp_ready(vp);
  Unroll<0, NK>::run([&](auto K) { dot8<K>(W[2 * K], W[2 * K + 1], vp, a); });
  dots_done(a);
#pragma unroll
  for (int j = 0; j < 4; ++j) u[j] = sum32(a[j], a[j + 4]);
}

__device__ __forceinline__ void ln_stats(const v2f (&e)[8], int lane, v2f& mean, v2f& rstd) {
  const v2f piv = {__uint_as_float(__builtin_amdgcn_readlane(__float_as_uint(e[0].x), 0)), __uint_as_float(__builtin_amdgcn_readlane(__float_as_uint(e[0].y), 0))};
  v2f d[8];
#pragma unroll
  for (int j = 0; j < 8; ++j) d[j] = e[j] - piv;
  const v2f s = ((d[0] + d[1]) + (d[2] + d[3])) + ((d[4] + d[5]) + (d[6] + d[7]));
  v2f qa = d[0] * d[0], qb = d[1] * d[1];
#pragma unroll
  for (int j = 2; j < 8; j += 2) { qa += d[j] * d[j]; qb += d[j + 1] * d[j + 1]; }
  qa += qb;
  float a = sum16(sum32(s.x, s.y), sum32(qa.x, qa.y));
  a += dppf<ROR8>(a); a += dppf<HMIR>(a); a += dppf<XOR2>(a); a += dppf<XOR1>(a);
  const auto t = __builtin_amdgcn_permlane16_swap(__float_as_uint(a), __float_as_uint(a), false, false);
  const float md = __uint_as_float(t[0]) * (1.f / 512.f), var = fmaxf(fmaf(-md, md, __uint_as_float(t[1]) * (1.f / 512.f)), 0.f);
  const v2f mv = rbf2(v2f{md + (lane < 32 ? piv.x : piv.y), var});
  const float rs = __builtin_amdgcn_rcpf(rbf(__builtin_amdgcn_sqrtf(rbf(mv.y + 1e-5f))));
  const auto m2 = __builtin_amdgcn_permlane32_swap(__float_as_uint(mv.x), __float_as_uint(mv.x), false, false);
  const auto r2 = __builtin_amdgcn_permlane32_swap(__float_as_uint(rs), __float_as_uint(rs), false, false);
  mean = v2f{__uint_as_float(m2[0]), __uint_as_float(m2[1])};
  rstd = v2f{__uint_as_float(r2[0]), __uint_as_float(r2[1])};
}
__device__ __forceinline__ v2f modulate(v2f x, v2f mean, v2f rstd, v2f scale1, v2f shift) { return rbf2(rbf2(rbf2(x - mean) * rstd) * scale1) + shift; }

__device__ __forceinline__ float qk(const v4u k, const v4u q) { return dot2(k.w, q.w, dot2(k.z, q.z, dot2(k.y, q.y, dot2(k.x, q.x, 0.f)))); }
__device__ __forceinline__ float tile_scores(float (&x)[16], int lane) {
#pragma unroll
  for (int j = 0; j < 8; ++j) x[j] = trstep<ROR8>(x[j], x[j + 8], lane & 8);
#pragma unroll
  for (int j = 0; j < 4; ++j) x[j] = trstep<HMIR>(x[j], x[j + 4], lane & 4);
#pragma unroll
  for (int j = 0; j < 2; ++j) x[j] = trstep<XOR2>(x[j], x[j + 2], lane & 2);
  const float d = trstep<XOR1>(x[0], x[1], lane & 1);
  return rbf(rbf(d) * 0.08838834764831845f);
}
template <int J> __device__ __forceinline__ v4u pv_diag(u32 phl, const v4u& sel) {
  const u32 t = dppu<BCAST + J>(phl);
  return v4u{__builtin_amdgcn_perm(t, t, sel.x), __builtin_amdgcn_perm(t, t, sel.y), __builtin_amdgcn_perm(t, t, sel.z), __builtin_amdgcn_perm(t, t, sel.w)};
}
#define HOLD(T) "pace%=:\n\ts_memrealtime s[40:41]\n\ts_waitcnt lgkmcnt(0)\n\ts_sub_u32 s40, s40, " T "\n\ts_cmp_ge_i32 s40, 0\n\t" \
                "s_cbranch_scc1 paced%=\n\ts_sleep 1\n\ts_branch pace%=\npaced%=:\n\t"
__device__ __forceinline__ void hold(u32 t) { asm volatile(HOLD("%0") :: "s"(t) : "s40", "s41", "scc"); }
__device__ __forceinline__ void dma4(rsrc_t rs, const v4u& vo, u32 soff, u32 lds) {
  u32 keep;
  asm volatile(
      "s_mov_b32 %0, m0\n\ts_mov_b32 m0, %7\n\ts_nop 0\n\t"
      "buffer_load_dwordx4 %1, %5, %6 offen nt lds\n\tbuffer_load_dwordx4 %2, %5, %6 offen offset:1024 nt lds\n\t"
      "buffer_load_dwordx4 %3, %5, %6 offen offset:2048 nt lds\n\tbuffer_load_dwordx4 %4, %5, %6 offen offset:3072 nt lds\n\t"
      "s_mov_b32 m0, %0"
      : "=&s"(keep) : "v"(vo.x), "v"(vo.y), "v"(vo.z), "v"(vo.w), "s"(rs), "s"(soff), "s"(lds) : "memory");
}
#define LOAD4(A, B, C, D, RS, SO) \
  "buffer_load_dwordx4 %0, " A ", " RS ", " SO " offen nt\n\tbuffer_load_dwordx4 %1, " B ", " RS ", " SO " offen offset:1024 nt\n\t" \
  "buffer_load_dwordx4 %2, " C ", " RS ", " SO " offen offset:2048 nt\n\tbuffer_load_dwordx4 %3, " D ", " RS ", " SO " offen offset:3072 nt\n\t"
__device__ __forceinline__ void load4_first(v4u* d, rsrc_t rs, const v4u& vo, u32 soff) {
  asm volatile(LOAD4("%4", "%5", "%6", "%7", "%8", "%9")
               : "=&v"(d[0]), "=&v"(d[1]), "=&v"(d[2]), "=&v"(d[3]) : "v"(vo.x), "v"(vo.y), "v"(vo.z), "v"(vo.w), "s"(rs), "s"(soff));
}
__device__ __forceinline__ void load4(v4u* d, rsrc_t rs, const v4u& vo, u32 soff) {
  asm volatile(LOAD4("%4", "%5", "%6", "%7", "%8", "%9")
               : "+v"(d[0]), "+v"(d[1]), "+v"(d[2]), "+v"(d[3]) : "v"(vo.x), "v"(vo.y), "v"(vo.z), "v"(vo.w), "s"(rs), "s"(soff));
}
template <int N> __device__ __forceinline__ void landed(v4u* d) {
  asm volatile("s_waitcnt vmcnt(%4)" : "+v"(d[0]), "+v"(d[1]), "+v"(d[2]), "+v"(d[3]) : "i"(N));
}
template <int NL, int ND> __device__ __forceinline__ void step4(v4u* d, v4u* u, rsrc_t rs, const v4u& vo, u32 soff, u32 t, u32 live) {
  asm volatile(
      "s_cmp_eq_u32 %15, 0\n\ts_cbranch_scc1 dead%=\n" HOLD("%14") LOAD4("%8", "%9", "%10", "%11", "%12", "%13")
      "s_waitcnt vmcnt(%16)\n\ts_branch done%=\n"
      "dead%=:\n\ts_waitcnt vmcnt(%17)\n"
      "done%=:"
      : "+v"(d[0]), "+v"(d[1]), "+v"(d[2]), "+v"(d[3]), "+v"(u[0]), "+v"(u[1]), "+v"(u[2]), "+v"(u[3])
      : "v"(vo.x), "v"(vo.y), "v"(vo.z), "v"(vo.w), "s"(rs), "s"(soff), "s"(t), "s"(live), "i"(NL), "i"(ND) : "s40", "s41", "scc");
}

__global__ void __launch_bounds__(512, 2) fused_block(Params p) {
  __shared__ __attribute__((aligned(16))) Lds L_;
  LAS Lds* L = (LAS Lds*)&L_;
  const int lane = threadIdx.x & 63, wave = __builtin_amdgcn_readfirstlane(threadIdx.x >> 6);
  const int r = lane >> 4, c = lane & 15;
  const int xl = blockIdx.x & 7, l = blockIdx.x >> 3;
  const int bh = (l >> 2) * 8 + xl, b = bh >> 5, h = bh & 31, s = l & 3;
  u32 t0 = now();

  if (wave >= 4) {
    const int w = wave - 4;
    const rsrc_t rk = mkrs(p.K + (size_t)bh * (8192 * 128), 8192 * 256), rv = mkrs(p.V + (size_t)bh * (8192 * 128), 8192 * 256);
    const u32 v0 = s * 16384 + w * 1024 + lane * 16;
    const v4u vo = {v0, v0 + 3072, v0 + 2 * 3072, v0 + 3 * 3072};
    v4u a[48];
    auto issue = [&](auto G) {
      constexpr int g = G, d0 = due_of(g), dF = due_of(PAUSE_G) + PAUSE_F + (((g - PAUSE_G) * FAST) >> 4), dR = dF - PAUSE_F + PAUSE_R;
      static_assert(g < 31 + LAG || dF <= d0, "caught up before the loop over V");
      if constexpr (g >= PAUSE_G && dF > d0) hold(t0 + (l < 12 ? dF : (l >= 24 && dR > d0) ? dR : d0));
      else hold(t0 + d0);
      if constexpr (g < 20) asm volatile("s_waitcnt vmcnt(%0)" :: "i"(g < DEPTH_G ? DEPTH - 4 : 28));
      if constexpr (g < 8) dma4(rk, vo, g * 65536, (u32)(uintptr_t)&L->kst[w][4 * g][0]);
      else if constexpr (g < 20) load4_first(a + 4 * slot_of(g), rk, vo, g * 65536);
      else load4(a + 4 * slot_of(g), g < 32 ? rk : rv, vo, (g & 31) * 65536);
      __builtin_amdgcn_sched_barrier(0);
    };
    __builtin_amdgcn_s_barrier();
    Unroll<0, 20>::run(issue);
    lds_wait(&L->cnt[C_Q], 1);
    t0 += __builtin_amdgcn_readfirstlane(L->cnt[C_SHIFT]);
    const v4u q = *(LAS v4u*)&L->qd[4 * c];
    float S[8], x[16];
    auto use = [&](auto G, auto LAST) {
      constexpr int g = G;
      if constexpr (g >= 8) landed<4 * (LAST - g)>(a + 4 * slot_of(g));
#pragma unroll
      for (int k = 0; k < 4; ++k) {
        if constexpr (g < 8) x[4 * (g & 3) + k] = qk(L->kst[w][4 * g + k][lane], q);
        else x[4 * (g & 3) + k] = qk(a[4 * slot_of(g) + k], q);
      }
      if constexpr ((g & 3) == 3) S[g >> 2] = tile_scores(x, lane);
      __builtin_amdgcn_sched_barrier(0);
    };
    Unroll<8, 12>::run([&](auto U) { use(U, std::integral_constant<int, 19>{}); });
    asm volatile("" ::: "memory");
    issue(std::integral_constant<int, 20>{});
    Unroll<0, 4>::run([&](auto U) { use(U, std::integral_constant<int, 20>{}); });
    issue(std::integral_constant<int, 21>{});
    Unroll<4, 8>::run([&](auto U) { use(U, std::integral_constant<int, 21>{}); });
    Unroll<22, 32 + LAG>::run([&](auto G) {
      issue(G);
      Unroll<used_after(G - 1) + 1, used_after(G) + 1>::run([&](auto U) { use(U, G); });
    });
    {
      float m = wave_max(fmaxf(fmaxf(fmaxf(S[0], S[1]), fmaxf(S[2], S[3])), fmaxf(fmaxf(S[4], S[5]), fmaxf(S[6], S[7]))));
      if (lane == 0) L->mw[w] = m;
      lds_arrive(&L->cnt[C_MAX]);
      lds_wait(&L->cnt[C_MAX], 4);
      const v4f m4 = *(LAS v4f*)L->mw;
      const float ml = fmaxf(fmaxf(m4.x, m4.y), fmaxf(m4.z, m4.w)) * 1.4426950408889634f;
      float ls = 0.f;
#pragma unroll
      for (int t = 0; t < 8; ++t) {
        const float pv = __builtin_amdgcn_exp2f(fmaf(S[t], 1.4426950408889634f, -ml));
        ls += pv;
        S[t] = __uint_as_float(pack2(pv, pv - rbf(pv)));
      }
      ls = wave_sum(ls);
      if (lane == 0) L->lw[w] = ls;
      lds_arrive(&L->cnt[C_SUM]);
    }
    v4f acc = {0.f, 0.f, 0.f, 0.f};
    v4u sel;
    {
      const u32 pick = (c & 8) ? 0x0302u : 0x0100u, own = (c & 1) ? (pick << 16) | 0x0c0cu : 0x0c0c0000u | pick;
#pragma unroll
      for (int d = 0; d < 4; ++d) sel[d] = ((c & 7) >> 1) == d ? own : 0x0c0c0c0cu;
    }
#pragma unroll 1
    for (int it = 0; it < 4; ++it) {
      const u32 tb = t0 + ((it * 8 * PACE) >> 4);
      Unroll<0, 8>::run([&](auto J) {
        constexpr int j = J, g = 32 + LAG + j;
        v4u bm[4];
        Unroll<0, 4>::run([&](auto K) { bm[K] = pv_diag<4 * (j & 3) + K>(__float_as_uint(S[j >> 2]), sel); pin(bm[K]); });
        step4<4 * LAG, 4 * (7 - j)>(a + 4 * slot_of(g), a + 4 * slot_of(32 + j), rv, vo, (LAG + j + 8 * it) * 65536, tb + due_of(g), j + LAG < 8 ? 1 : 3 - it);
#pragma unroll
        for (int k = 0; k < 4; ++k)
          acc = __builtin_amdgcn_mfma_f32_16x16x32_bf16(__builtin_bit_cast(bf16x8, a[4 * slot_of(32 + j) + k]), __builtin_bit_cast(bf16x8, bm[k]), acc, 0, 0, 0);
        pin(acc);
        __builtin_amdgcn_sched_barrier(0);
      });
#pragma unroll
      for (int t = 0; t < 6; ++t) S[t] = S[t + 2];
    }
    LAS float* o = &L->op[w][32 * r + 2 * (c & 8) + (c & 7)];
    const float o0 = acc.x + dppf<ROR8>(acc.x), o1 = acc.y + dppf<ROR8>(acc.y), o2 = acc.z + dppf<ROR8>(acc.z), o3 = acc.w + dppf<ROR8>(acc.w);
    o[0] = (c & 8) ? o2 : o0; o[8] = (c & 8) ? o3 : o1;
    wg_barrier();
    return;
  }

  const int w = wave, t = w * 64 + lane;
  const rsrc_t rs = mkrs(p.ws, WS_END), rm = mkrs(p.wmod, 512 * 3072 * 2);
  const bool first = l >= 16 && w >= 2;
  v2u c0 = {0u, 0u}, c1 = c0;
  v4u W[32];
  if (first) {
    const int row = 64 * xl + 32 * (w - 2) + 4 * (lane >> 3);
    c0 = ((const GAS v2u*)p.c)[row >> 2]; c1 = ((const GAS v2u*)p.c)[128 + (row >> 2)];
#pragma unroll
    for (int k = 0; k < 4; ++k) W[k] = ldw(rm, row * 6144 + (lane & 7) * 16, k * 6144 + (l - 16) * 128);
  }
  const u32 epoch = __hip_atomic_load((GAS u32*)(p.ws + WS_EPOCH), __ATOMIC_RELAXED, __HIP_MEMORY_SCOPE_AGENT);
  const int j8 = 2 * s + (w & 1);
  v4u xs = {0u, 0u, 0u, 0u};
  u32 xo = 0;
  if (w < 2) {
    xs = ((const GAS v4u*)p.x)[b * 64 + lane];
    xo = ((const GAS u32*)p.x)[b * 256 + 32 * j8 + 8 * r + (c & 7)];
    const rsrc_t rq = mkrs(p.wq, 512 * 4096 * 2);
#pragma unroll
    for (int k = 0; k < 16; ++k) W[k] = ldw(rq, (64 * j8 + 16 * r) * 8192 + h * 256 + c * 16, k * 8192);
  }
  __builtin_amdgcn_sched_barrier(0);
  if (threadIdx.x < 16) L->cnt[threadIdx.x] = 0;
  wg_barrier();
  __builtin_amdgcn_sched_barrier(0);
  if (w == 3 && l == 12) {
    u64 pc;
    asm volatile("s_getpc_b64 %0" : "=s"(pc));
#pragma unroll
    for (int k = 0; k < CODE_KB; ++k) W[k] = ((const GAS v4u*)(pc & ~127ull))[k * 64 + lane];
#pragma unroll
    for (int k = 0; k < CODE_KB; ++k) pin(W[k]);
  }
  const u32 tag = ((u32)__builtin_amdgcn_readfirstlane(epoch) | 1u) + 2u;
  if (l == 0 && w == 3) publish(rs, lane == 0 ? WS_T + xl * 16 : OOB, t0, t0, tag);
  if (first) {
    float v[16] = {0.f, 0.f, 0.f, 0.f, 0.f, 0.f, 0.f, 0.f, 0.f, 0.f, 0.f, 0.f, 0.f, 0.f, 0.f, 0.f};
    rows2(W[0], W[1], silu2(c0.x), silu2(c1.x), v);
    rows2(W[2], W[3], silu2(c0.y), silu2(c1.y), v);
#pragma unroll
    for (int j = 0; j < 8; ++j) v[j] = sum32(v[j], v[j + 8]);
#pragma unroll
    for (int j = 0; j < 4; ++j) v[j] = sum16(v[j], v[j + 4]);
    L->red1[w][lane] = v2f{trstep<ROR8>(v[0], v[2], lane & 8), trstep<ROR8>(v[1], v[3], lane & 8)};
    if (last_of(&L->cnt[C_MP], 2, lane)) {
      const int src = (lane & 32) + (lane & 3) * 8 + ((lane >> 2) & 7);
      publish(rs, WS_MP + ((l - 16) * 8 + xl) * 1024 + lane * 16, L->red1[2][src] + L->red1[3][src], tag);
    }
  }
  if (w < 2) {
    v2f mean, rstd;
    {
      v2f e[8];
#pragma unroll
      for (int k = 0; k < 4; ++k) { e[2 * k] = v2f{bflo(xs[k]), bflo(xs[k])}; e[2 * k + 1] = v2f{bfhi(xs[k]), bfhi(xs[k])}; }
      ln_stats(e, lane, mean, rstd);
      pin(mean); pin(rstd);
    }
#pragma unroll
    for (int k = 0; k < 8; ++k) { interleave(W[2 * k], W[2 * k + 1]); pin(W[2 * k]); pin(W[2 * k + 1]); }
    u32 off[8];
#pragma unroll
    for (int k = 0; k < 8; ++k) off[k] = WS_MP + (((c & 8) + j8) * 8 + k) * 1024 + b * 512 + (8 * r + (c & 7)) * 16;
    v4u val[8];
    hold(t0 + MOD_T);
    gather<8, 2>(rs, off, val, tag, t0);
    v2f m = payload(val[0]);
#pragma unroll
    for (int k = 1; k < 8; ++k) m += payload(val[k]);
    const u32 own = pack2(m.x, m.y), oth = dppu<ROR8>(own);
    const v2f hv = modulate(unpack2(xo), mean, rstd, rbf2(unpack2((c & 8) ? own : oth) + 1.f), unpack2((c & 8) ? oth : own));
    float u[4];
    gemv<8>(W, pack2(hv.x, hv.y), u);
    L->red1[w][lane] = v2f{sum16(u[0], u[2]), sum16(u[1], u[3])};
    if (last_of(&L->cnt[C_QP], 2, lane)) {
      publish(rs, WS_QP + (bh * 4 + s) * 1024 + lane * 16, L->red1[0][lane] + L->red1[1][lane], tag);
      lds_arrive(&L->cnt[C_QPUB]);
    }
  } else if (w == 2) {
    const u32 o0 = WS_QP + bh * 4096 + lane * 16, off[5] = {o0, o0 + 1024, o0 + 2048, o0 + 3072, WS_T + (lane & 7) * 16};
    v4u val[5];
    lds_wait(&L->cnt[C_QPUB], 1);
    __builtin_amdgcn_s_sleep(D_Q);
    gather<5, 2>(rs, off, val, tag, t0);
    int d = (int)(val[4].x - t0);
    d = min(d, (int)dppu<XOR1>(d)); d = min(d, (int)dppu<XOR2>(d)); d = min(d, (int)dppu<HMIR>(d));
    L->cnt[C_SHIFT] = max(min(d, 0), -200);
    const v2f qv = (payload(val[0]) + payload(val[1])) + (payload(val[2]) + payload(val[3]));
    L->qd[4 * c + r] = pack2(qv.x, qv.y);
    lds_arrive(&L->cnt[C_Q]);
  }

  const int fstart = (43 * xl + 3) >> 2, fg = fstart + l;
  const bool ffn = fg < (43 * (xl + 1) + 3) >> 2;
  v4u F[8];
  v4u W2[4];
#pragma unroll
  for (int k = 0; k < 8; ++k) F[k] = v4u{0u, 0u, 0u, 0u};
#pragma unroll
  for (int k = 0; k < 4; ++k) W2[k] = v4u{0u, 0u, 0u, 0u};
  if (l >= 24) {
    const int rg = xl * 8 + l - 24, row = 128 * w + 8 * (lane >> 2);
    hold(t0 + STRIP_T);
    const v4u ca = ((const GAS v4u*)p.c)[row >> 3], cb = ((const GAS v4u*)p.c)[64 + (row >> 3)];
#pragma unroll
    for (int k = 0; k < 8; ++k) W[k] = ldw(rm, row * 6144 + (lane & 3) * 16, k * 6144 + 2048 + rg * 64);
    float v[16] = {0.f, 0.f, 0.f, 0.f, 0.f, 0.f, 0.f, 0.f, 0.f, 0.f, 0.f, 0.f, 0.f, 0.f, 0.f, 0.f};
#pragma unroll
    for (int k = 0; k < 4; ++k) rows2(W[2 * k], W[2 * k + 1], silu2(ca[k]), silu2(cb[k]), v);
#pragma unroll
    for (int j = 0; j < 8; ++j) v[j] = sum32(v[j], v[j + 8]);
#pragma unroll
    for (int j = 0; j < 4; ++j) { v[j] = sum16(v[j], v[j + 4]); v[j] += dppf<ROR8>(v[j]); v[j] += dppf<ROR4>(v[j]); }
    if ((lane & 12) == 0) L->red1b[w][4 * r + (lane & 3)] = v4f{v[0], v[1], v[2], v[3]};
    if (last_of(&L->cnt[C_MOD], 4, lane)) {
      const int src = (c & 8) + (c & 1) * 4 + ((c >> 1) & 3);
      const v4f a = (L->red1b[0][src] + L->red1b[1][src]) + (L->red1b[2][src] + L->red1b[3][src]);
      publish(rs, lane < 16 ? WS_MOD + (c >> 3) * 8192 + (8 * rg + (c & 7)) * 16 : OOB, pack2(a.x, a.y), pack2(a.z, a.w), tag);
    }
  } else if (ffn) {
    const rsrc_t r1 = mkrs(p.w1, 512 * 1376 * 2), r3 = mkrs(p.w3, 512 * 1376 * 2), r2 = mkrs(p.w2, 1376 * 512 * 2);
    hold(t0 + STRIP_T);
#pragma unroll
    for (int k = 0; k < 8; ++k) F[k] = ldw(k < 4 ? r1 : r3, (128 * w + (lane >> 1)) * 2752 + (lane & 1) * 16, (k & 3) * 32 * 2752 + fg * 32);
#pragma unroll
    for (int k = 0; k < 4; ++k) W2[k] = ldw(r2, r * 4096 + w * 256 + c * 16, (16 * fg + k) * 1024);
    LAS v4s* tr = (LAS v4s*)&L->stage[w][0] + (8 * r + (c >> 2)) * 4 + (c & 3);
#pragma unroll
    for (int k = 0; k < 8; ++k) {
      L->stage[w][lane] = F[k];
      asm volatile("" ::: "memory");
      const v2u lo = __builtin_bit_cast(v2u, __builtin_amdgcn_ds_read_tr16_b64_v4i16(tr));
      const v2u hi = __builtin_bit_cast(v2u, __builtin_amdgcn_ds_read_tr16_b64_v4i16(tr + 16));
      F[k] = v4u{lo.x, lo.y, hi.x, hi.y};
      asm volatile("" : "+v"(F[k]) :: "memory");
    }
    interleave(W2[0], W2[1]); interleave(W2[2], W2[3]);
  }
  lds_wait(&L->cnt[C_Q], 1);
  t0 += __builtin_amdgcn_readfirstlane(L->cnt[C_SHIFT]);

  const bool red = l < 4;
  const int j = xl * 4 + (l & 3), rp = 64 * (j >> 3) + 16 * (j & 3) + 4 * (lane & 3) + 2 * ((j >> 2) & 1) + ((lane >> 2) & 1);
  const int tp = w * 64 + 16 * ((lane >> 3) & 3) + 4 * (lane & 3) + 2 * (lane >> 5) + ((lane >> 2) & 1);
  v2f sc0 = {0.f, 0.f}, sc1 = sc0, sh0 = sc0, sh1 = sc0;
  float xr = 0.f, gar = 0.f, gbr = 0.f, mloc = 0.f;
  {
    const rsrc_t ro = mkrs(p.wo, 4096 * 512 * 2);
    Unroll<0, 16>::run([&](auto K) {
      hold(t0 + WO_T + K * WO_DT);
      W[2 * K] = ldw(ro, (h * 128 + 2 * r) * 1024 + w * 256 + c * 16, K * 8192);
      W[2 * K + 1] = ldw(ro, (h * 128 + 2 * r) * 1024 + w * 256 + c * 16, K * 8192 + 1024);
      __builtin_amdgcn_sched_barrier(0);
      if constexpr (K == MOD_K) {
        if (ffn) {
          const u32 modb = WS_MOD + (1 + (lane >> 5)) * 2048 + (32 * w + (lane & 31)) * 16;
          const u32 base = WS_MOD + (r >> 1) * 8192 + (rp >> 1) * 16;
          const u32 off[4] = {modb, modb + 8192, (red && w == 0) ? base : OOB, (red && w == 0) ? base + 3 * 2048 : OOB};
          v4u val[4];
          const u32 xv = ((const GAS u32*)p.x)[(r >> 1) * 256 + rp];
          gather<4, 16, 2>(rs, off, val, tag, t0);
          const int src = (tp & 62) * 2;
          auto fetch = [&](int from, const v4u& u) {
            u32 lo = __builtin_amdgcn_ds_bpermute(from, u.x), hi = __builtin_amdgcn_ds_bpermute(from, u.z);
            pin(lo); pin(hi);
            return (tp & 1) ? hi : lo;
          };
          const u32 h0 = fetch(src, val[0]), h1 = fetch(src, val[1]), g0 = fetch(src + 128, val[0]), g1 = fetch(src + 128, val[1]);
          sc0 = rbf2(v2f{bflo(g0), bflo(g1)} + 1.f); sc1 = rbf2(v2f{bfhi(g0), bfhi(g1)} + 1.f);
          sh0 = v2f{bflo(h0), bflo(h1)}; sh1 = v2f{bfhi(h0), bfhi(h1)};
          const u32 ga = (rp & 1) ? val[2].z : val[2].x, gb = (rp & 1) ? val[3].z : val[3].x;
          xr = (r & 1) ? bfhi(xv) : bflo(xv); gar = (r & 1) ? bfhi(ga) : bflo(ga); gbr = (r & 1) ? bfhi(gb) : bflo(gb);
        }
        __builtin_amdgcn_sched_barrier(0);
      }
      if constexpr (K == ML_K) {
        lds_wait(&L->cnt[C_SUM], 4);
        const v4f m4 = *(LAS v4f*)L->mw, l4 = *(LAS v4f*)L->lw;
        mloc = fmaxf(fmaxf(m4.x, m4.y), fmaxf(m4.z, m4.w));
        publish(rs, t == 0 ? WS_ML + (bh * 4 + s) * 16 : OOB, v2f{mloc, (l4.x + l4.y) + (l4.z + l4.w)}, tag);
        __builtin_amdgcn_sched_barrier(0);
      }
    });
  }
#pragma unroll
  for (int k = 0; k < 16; ++k) { interleave(W[2 * k], W[2 * k + 1]); pin(W[2 * k]); pin(W[2 * k + 1]); }
  float scale;
  {
    const u32 off[1] = {WS_ML + (bh * 4 + (lane & 3)) * 16};
    v4u val[1];
    gather<1, 16>(rs, off, val, tag, t0);
    const v2f ml = payload(val[0]);
    float M = fmaxf(ml.x, dppf<XOR1>(ml.x)); M = fmaxf(M, dppf<XOR2>(M));
    float Ls = ml.y * __builtin_amdgcn_exp2f((ml.x - M) * 1.4426950408889634f);
    Ls += dppf<XOR1>(Ls); Ls += dppf<XOR2>(Ls);
    scale = __builtin_amdgcn_exp2f((mloc - M) * 1.4426950408889634f) / Ls;
  }

  u32 o_y = (r & 1) ? OOB : WS_Y + ((8 * w + 2 * r + (c >> 2)) * 256 + bh * 4 + s) * 64 + (c & 3) * 16;
  u32 o_yg = WS_Y + j * 16384 + w * 4096 + lane * 16;
  u32 o_x = (lane >> 3) < REP_X1 ? WS_X1 + (lane >> 3) * 4096 + j * 128 + (lane & 7) * 16 : OOB;
  u32 o_xg[4];
#pragma unroll
  for (int k = 0; k < 4; ++k) { o_xg[k] = WS_X1 + (xl & (REP_X1 - 1)) * 4096 + lane * 16 + ((w + k) & 3) * 1024; pin(o_xg[k]); }
  u32 o_o = WS_OP + ((8 * w + 4 * (r >> 1) + (c >> 2)) * 96 + fg) * 128 + (r & 1) * 64 + (c & 3) * 16;
  u32 o_og[3];
  o_og[0] = WS_OP + j * 12288 + w * 3072 + lane * 16;
  o_og[1] = (w < 3 || (lane >> 3) < NFFN - 80) ? o_og[0] + 1024 : OOB;
  o_og[2] = w < 3 ? o_og[0] + 2048 : OOB;
  u32 o_e = (j == 0 && lane == 0) ? WS_EPOCH : OOB;
  u32 o_out = (lane & 8) == 0 ? ((r >> 1) * 512 + 2 * rp + (r & 1)) * 2 : OOB;
  const rsrc_t rout = mkrs(p.out, 2048);
  LAS v2f* a_o = (LAS v2f*)&L->op[0][8 * c + 2 * r];
  LAS float* a_rw = &L->ry[w][4 * r + (lane & 3)];
  LAS float* a_rr = &L->ry[r & 2][2 * (lane & 4) + 4 * (r & 1) + (lane & 3)];
  LAS u32* a_hw = &L->h2[0][tp];
  LAS v4u* a_hr = (LAS v4u*)&L->h2[c & 1][64 * w + 4 * r];
  LAS float* a_gw = &L->red2[lane][w];
  LAS v4f* a_gr = (LAS v4f*)L->red2[(c & 4) * 4 + 4 * r + (c & 3)];
  LAS float* a_ow = &L->ro[w][8 * r + (lane & 7)];
  LAS float* a_or = &L->ro[0][8 * r + (lane & 7)];
  pin(o_y); pin(o_yg); pin(o_x); pin(o_o); pin(o_og[0]); pin(o_og[1]); pin(o_og[2]); pin(o_e); pin(o_out);
  pin(scale); pin(sc0); pin(sc1); pin(sh0); pin(sh1); pin(xr); pin(gar); pin(gbr);
  pin(a_o); pin(a_rw); pin(a_rr); pin(a_hw); pin(a_hr); pin(a_gw); pin(a_gr); pin(a_ow); pin(a_or);

  wg_barrier();
  {
    const v2f o = ((a_o[0] + a_o[64]) + (a_o[128] + a_o[192])) * scale;
    float u[4];
    gemv<16>(W, pack2(o.x, o.y), u);
    publish(rs, o_y, pack2(sum16(u[0], u[0]), sum16(u[1], u[1])), pack2(sum16(u[2], u[2]), sum16(u[3], u[3])), tag);
  }
  if (ffn) {
    float x1r = 0.f;
    {
      const u32 off[4] = {o_yg, o_yg + 1024, o_yg + 2048, o_yg + 3072};
      v4u val[4];
      __builtin_amdgcn_s_sleep(D_Y);
      gather<4, 0>(rs, off, val, tag, t0);
      const v2f lo = (unpack2(val[0].x) + unpack2(val[1].x)) + (unpack2(val[2].x) + unpack2(val[3].x));
      const v2f hi = (unpack2(val[0].z) + unpack2(val[1].z)) + (unpack2(val[2].z) + unpack2(val[3].z));
      float a = sum16(sum32(lo.x, hi.x), sum32(lo.y, hi.y));
      a += dppf<ROR8>(a); a += dppf<ROR4>(a);
      a_rw[0] = a;
      wg_barrier();
      if (red && w == 0) {
        x1r = rbf(xr + rbf(gar * rbf(a_rr[0] + a_rr[16])));
        const auto e = __builtin_amdgcn_permlane16_swap(__float_as_uint(x1r), __float_as_uint(x1r), false, false);
        const u32 pr = (e[0] >> 16) | e[1];
        const auto bb = __builtin_amdgcn_permlane32_swap(pr, pr, false, false);
        publish(rs, o_x, bb[0], bb[1], tag);
        __builtin_amdgcn_raw_buffer_store_b32(tag, rs, o_e, 0, AUX_SC1);
        __builtin_amdgcn_s_sleep(D_XR);
      } else __builtin_amdgcn_s_sleep(D_X);
    }
    v4u val[4];
    gather<4, 0>(rs, o_xg, val, tag, t0);
    v2f e[8], mean, rstd;
#pragma unroll
    for (int k = 0; k < 4; ++k) { e[2 * k] = v2f{bflo(val[k].x), bflo(val[k].z)}; e[2 * k + 1] = v2f{bfhi(val[k].x), bfhi(val[k].z)}; }
    ln_stats(e, lane, mean, rstd);
    const v2f ha = modulate(e[0], mean, rstd, sc0, sh0), hb = modulate(e[1], mean, rstd, sc1, sh1);
    a_hw[0] = pack2(ha.x, hb.x); a_hw[256] = pack2(ha.y, hb.y);
    asm volatile("" ::: "memory");
    v4u A[4];
#pragma unroll
    for (int k = 0; k < 4; ++k) A[k] = a_hr[4 * k];
    __builtin_amdgcn_sched_barrier(0);
    v4f dg = {0.f, 0.f, 0.f, 0.f}, du = dg;
#pragma unroll
    for (int k = 0; k < 4; ++k) {
      dg = __builtin_amdgcn_mfma_f32_16x16x32_bf16(__builtin_bit_cast(bf16x8, A[k]), __builtin_bit_cast(bf16x8, F[k]), dg, 0, 0, 0);
      du = __builtin_amdgcn_mfma_f32_16x16x32_bf16(__builtin_bit_cast(bf16x8, A[k]), __builtin_bit_cast(bf16x8, F[4 + k]), du, 0, 0, 0);
    }
    const float lo = (lane & 16) ? dg.y : dg.x, hi = (lane & 16) ? du.y : du.x;
    a_gw[0] = (lane & 32) ? hi : lo;
    wg_barrier();
    const v4f g4 = a_gr[0], u4 = a_gr[32];
    const v2f gu = rbf2(v2f{(g4.x + g4.y) + (g4.z + g4.w), (u4.x + u4.y) + (u4.z + u4.w)});
    const float hh = rbf(silu(gu.x)) * gu.y;
    u32 hp = pack2(hh, dppf<XOR1>(hh));
    float o[16] = {0.f, 0.f, 0.f, 0.f, 0.f, 0.f, 0.f, 0.f, 0.f, 0.f, 0.f, 0.f, 0.f, 0.f, 0.f, 0.f};
    dpp_ready(hp);
    dot8<0>(W2[0], W2[1], hp, o); dot8<4>(W2[0], W2[1], hp, o + 8); dot8<2>(W2[2], W2[3], hp, o); dot8<6>(W2[2], W2[3], hp, o + 8);
    dots_done(o); dots_done(o + 8);
#pragma unroll
    for (int k = 0; k < 4; ++k) { o[k] = sum32(o[k], o[k + 4]); o[8 + k] = sum32(o[8 + k], o[12 + k]); }
    publish(rs, o_o, pack2(sum16(o[0], o[2]), sum16(o[1], o[3])), pack2(sum16(o[8], o[10]), sum16(o[9], o[11])), tag);
    if (red) {
      v4u va[3];
      __builtin_amdgcn_s_sleep(D_O);
      gather<3, 0, 1>(rs, o_og, va, tag, t0);
      const v2f b0 = unpack2(va[0].x) + unpack2(va[1].x) + unpack2(va[2].x), b1 = unpack2(va[0].z) + unpack2(va[1].z) + unpack2(va[2].z);
      float a = sum16(sum32(b0.x, b1.x), sum32(b0.y, b1.y));
      a += dppf<ROR8>(a);
      a_ow[0] = a;
      wg_barrier();
      if (w == 0) {
        const float res = x1r + rbf(gbr * rbf((a_or[0] + a_or[32]) + (a_or[64] + a_or[96])));
        __builtin_amdgcn_raw_buffer_store_b16((u16)pack2(res, res), rout, o_out, 0, 0);
      }
    }
  }
}

extern "C" void kernel_launch(void* const* d_in, const int* in_sizes, int n_in, void* d_out, int out_size, void* d_ws, size_t ws_size, hipStream_t stream) {
  Params p;
  p.x = (const u16*)d_in[0]; p.K = (const u16*)d_in[1]; p.V = (const u16*)d_in[2]; p.wq = (const u16*)d_in[3]; p.wo = (const u16*)d_in[4];
  p.w1 = (const u16*)d_in[6]; p.w3 = (const u16*)d_in[7]; p.w2 = (const u16*)d_in[8]; p.c = (const u16*)d_in[9]; p.wmod = (const u16*)d_in[10];
  p.out = (u16*)d_out; p.ws = (unsigned char*)d_ws;
  hipLaunchKernelGGL(fused_block, dim3(256), dim3(512), 0, stream, p);
}
```
